```python
import torch

def _softmax(x, axis=-1):
    return torch.softmax(x, dim=axis)

def _gelu(x):
    return torch.nn.functional.gelu(x, approximate='tanh')

def _silu(x):
    return torch.nn.functional.silu(x)

def _sigmoid(x):
    return torch.sigmoid(x)

def _layernorm(x, axis=-1, eps=1e-05):
    m = x.mean(dim=axis, keepdim=True)
    v = x.var(dim=axis, keepdim=True, unbiased=False)
    return (x - m) / torch.sqrt(v + eps)

def _scatter_add(tgt, idx, src, axis=0):
    return tgt.clone().index_add_(axis, idx, src)

def _segment_sum(src, seg, n):
    out = torch.zeros((n,) + src.shape[1:], dtype=src.dtype)
    return out.index_add_(0, seg, src)

def _pad(x, pw):
    flat = tuple((v for pair in reversed(pw) for v in pair))
    return torch.nn.functional.pad(x, flat)
kernel_name = 'adaLN_cond__sliding_block_cached__geglu_ffn__decode'

def setup_inputs():

    _w = lambda a: (lambda t: t.to(torch.bfloat16) if t.is_floating_point() else t)(torch.as_tensor(a))

    def _A_setup():
        B, Sq, D = (1, 1, 640)
        Hq, Hkv, Sk, Dh = (32, 32, 32768, 192)
        assert Hq % Hkv == 0, (Hq, Hkv)
        return {'x': _w((lambda *s: torch.randn(*s, dtype=torch.bfloat16) * 0.1)(B, Sq, D)), 'k_cache': _w((lambda *s: torch.randn(*s, dtype=torch.bfloat16) * 0.8)(B, Hkv, Sk, Dh)), 'v_cache': _w((lambda *s: torch.randn(*s, dtype=torch.bfloat16) * 1.6)(B, Hkv, Sk, Dh)), 'wq': _w((lambda *s: torch.randn(*s, dtype=torch.bfloat16) * 0.8)(D, Hq * Dh) * D ** (-0.5)), 'wo': _w((lambda *s: torch.randn(*s, dtype=torch.bfloat16) * 1.6)(Hq * Dh, D) * (Hq * Dh) ** (-0.5))}
    _A_in = _A_setup()

    def _B_setup():
        B, S, D, F = (1, 1, 640, 1536)
        return {'x': _w((lambda *s: torch.randn(*s, dtype=torch.bfloat16) * 0.1)(B, S, D)), 'w_gate': _w((lambda *s: torch.randn(*s, dtype=torch.bfloat16) * 0.1)(D, F)), 'w_up': _w((lambda *s: torch.randn(*s, dtype=torch.bfloat16) * 0.1)(D, F)), 'w_down': _w((lambda *s: torch.randn(*s, dtype=torch.bfloat16) * 0.1)(F, D))}
    _B_in = _B_setup()
    return {'A_x': _A_in['x'], 'A_k_cache': _A_in['k_cache'], 'A_v_cache': _A_in['v_cache'], 'A_wq': _A_in['wq'], 'A_wo': _A_in['wo'], 'B_x': _B_in['x'], 'B_w_gate': _B_in['w_gate'], 'B_w_up': _B_in['w_up'], 'B_w_down': _B_in['w_down'], 'c': _w((lambda *s: torch.randn(*s, dtype=torch.bfloat16) * 0.8)(1, 640) * 8.0), 'w_mod': _w((lambda *s: torch.randn(*s, dtype=torch.bfloat16) * 0.8)(640, 6 * 640) * 640 ** (-0.5) * 8.0)}

def reference(A_x, A_k_cache, A_v_cache, A_wq, A_wo, B_x, B_w_gate, B_w_up, B_w_down, c, w_mod):

    def _A_ref(x, k_cache, v_cache, wq, wo):
        B, Sq, D = x.shape
        Hq, Dh = (32, 192)
        Hkv, Sk = (k_cache.shape[1], k_cache.shape[2])
        W = min(4096, max(256, Sk // 4))
        rep = Hq // Hkv
        q = (lambda x, *d: x.permute(*d))((x @ wq).reshape(B, Sq, Hq, Dh), 0, 2, 1, 3)
        k = (lambda x, n, axis: torch.repeat_interleave(x, n, dim=axis))(k_cache[:, :, Sk - W:, :], rep, axis=1)
        v = (lambda x, n, axis: torch.repeat_interleave(x, n, dim=axis))(v_cache[:, :, Sk - W:, :], rep, axis=1)
        scores = torch.einsum('bhqd,bhkd->bhqk', q, k) * Dh ** (-0.5)
        p = _softmax(scores, axis=-1)
        o = torch.einsum('bhqk,bhkd->bhqd', p, v)
        return (lambda x, *d: x.permute(*d))(o, 0, 2, 1, 3).reshape(B, Sq, Hq * Dh) @ wo

    def _B_ref(x, w_gate, w_up, w_down):
        return _gelu(x @ w_gate) * (x @ w_up) @ w_down
    x = A_x
    mod = (_silu(c) @ w_mod).reshape(c.shape[0], 6, 1, -1)
    sa, ca, ga, sb, cb, gb = (mod[:, 0].clone(), mod[:, 1].clone(), mod[:, 2].clone(), mod[:, 3].clone(), mod[:, 4].clone(), mod[:, 5].clone())
    h = _layernorm(x, axis=-1) * (1 + ca) + sa
    x = x + ga * _A_ref(h, A_k_cache, A_v_cache, A_wq, A_wo)
    h = _layernorm(x, axis=-1) * (1 + cb) + sb
    x = x + gb * _B_ref(h, B_w_gate, B_w_up, B_w_down)
    return x
```

```cpp
#include <hip/hip_runtime.h>
#include <hip/hip_bf16.h>
#include <stdint.h>
#define MIMIC 0
typedef unsigned short u16;
typedef unsigned v4u __attribute__((ext_vector_type(4)));
constexpr int D=640, H=32, DH=192, SK=32768, W=4096, F=1536, NS=8, KPS=W/NS;

__device__ __forceinline__ float bf2f(u16 b){ return __uint_as_float(((unsigned)b)<<16); }
__device__ __forceinline__ float rbf(float f){
  return f;
}
__device__ __forceinline__ u16 f2bf(float f){ unsigned u=__float_as_uint(f); u += 0x7FFFu + ((u>>16)&1u); return (u16)(u>>16); }

__global__ void k_mod(const u16* c, const u16* wmod, float* mod){
  __shared__ float s[D]; __shared__ float red[4][64];
  for(int i=threadIdx.x;i<D;i+=256){ float v=bf2f(c[i]); s[i]=rbf(v/(1.f+__expf(-v))); }
  __syncthreads();
  int n=blockIdx.x*64+(threadIdx.x&63), w=threadIdx.x>>6; float a=0;
  for(int k=w*160;k<w*160+160;k++) a+=s[k]*bf2f(wmod[(size_t)k*3840+n]);
  red[w][threadIdx.x&63]=a; __syncthreads();
  if(w==0){ int l=threadIdx.x; mod[n]=rbf(red[0][l]+red[1][l]+red[2][l]+red[3][l]); }
}
__device__ void ln_mod(const float* x, const float* sh, const float* sc, float* h, float* tmp){
  float m=0; for(int i=0;i<D;i++) m+=x[i]; m/=D; float mr=rbf(m);
  float v=0; for(int i=0;i<D;i++){ float d=x[i]-m; v+=d*d; } v/=D; v=rbf(v);
  float den=rbf(sqrtf(rbf(v+1e-5f)));
  for(int i=threadIdx.x;i<D;i+=256){ float n=rbf(rbf(x[i]-mr)/den); h[i]=rbf(rbf(n*rbf(1.f+sc[i]))+sh[i]); }
}
__global__ void k_q(const u16* x, const float* mod, const u16* wq, float* q){
  __shared__ float xs[D], h[D]; __shared__ float red[4][64];
  for(int i=threadIdx.x;i<D;i+=256) xs[i]=bf2f(x[i]);
  __syncthreads();
  ln_mod(xs, mod, mod+640, h, nullptr); __syncthreads();
  int n=blockIdx.x*64+(threadIdx.x&63), w=threadIdx.x>>6; float a=0;
  for(int k=w*160;k<w*160+160;k++) a+=h[k]*bf2f(wq[(size_t)k*6144+n]);
  red[w][threadIdx.x&63]=a; __syncthreads();
  if(w==0){ int l=threadIdx.x; q[n]=rbf(red[0][l]+red[1][l]+red[2][l]+red[3][l]); }
}
__global__ void k_attn(const float* q, const u16* K, const u16* V, float* o){
  __shared__ float qs[DH]; __shared__ float sc[W]; __shared__ float red[1024];
  int h=blockIdx.x, t=threadIdx.x;
  if(t<DH) qs[t]=q[h*DH+t];
  __syncthreads();
  const u16* Kh=K+((size_t)h*SK+(SK-W))*DH; const u16* Vh=V+((size_t)h*SK+(SK-W))*DH;
  float mx=-INFINITY;
  for(int j=t;j<W;j+=1024){ float a=0; for(int d=0;d<DH;d++) a+=qs[d]*bf2f(Kh[(size_t)j*DH+d]);
    a=rbf(rbf(a)*0.07216878364870322f); sc[j]=a; mx=fmaxf(mx,a);}
  red[t]=mx; __syncthreads();
  for(int s=512;s>0;s>>=1){ if(t<s) red[t]=fmaxf(red[t],red[t+s]); __syncthreads(); }
  mx=red[0]; __syncthreads();
  float l=0; for(int j=t;j<W;j+=1024){ float p=__expf(sc[j]-mx); sc[j]=p; l+=p; }
  red[t]=l; __syncthreads();
  for(int s=512;s>0;s>>=1){ if(t<s) red[t]+=red[t+s]; __syncthreads(); }
  l=red[0]; __syncthreads();
  for(int j=t;j<W;j+=1024) sc[j]=rbf(sc[j]/l);
  __syncthreads();
  int d=t%192, g=t/192; float a=0;
  if(g<5){ for(int j=g;j<W;j+=5) a+=sc[j]*bf2f(Vh[(size_t)j*DH+d]); }
  red[t]=a; __syncthreads();
  if(t<192) o[h*DH+t]=rbf(red[t]+red[t+192]+red[t+384]+red[t+576]+red[t+768]);
}
__global__ void k_wo(const float* o, const u16* wo, const u16* x, const float* mod, float* x1){
  __shared__ float red[16][64];
  int n=blockIdx.x*64+(threadIdx.x&63), w=threadIdx.x>>6; float a=0;
  for(int k=w*384;k<w*384+384;k++) a+=o[k]*bf2f(wo[(size_t)k*640+n]);
  red[w][threadIdx.x&63]=a; __syncthreads();
  if(w==0){ int l=threadIdx.x; float s=0; for(int i=0;i<16;i++) s+=red[i][l]; x1[n]=rbf(bf2f(x[n])+rbf(mod[1280+n]*rbf(s))); }
}
__global__ void k_gu(const float* x1, const float* mod, const u16* wg, const u16* wu, float* g){
  __shared__ float xs[D], h[D]; __shared__ float red[4][64], red2[4][64];
  for(int i=threadIdx.x;i<D;i+=256) xs[i]=x1[i];
  __syncthreads();
  ln_mod(xs, mod+1920, mod+2560, h, nullptr); __syncthreads();
  int n=blockIdx.x*64+(threadIdx.x&63), w=threadIdx.x>>6; float a=0,b=0;
  for(int k=w*160;k<w*160+160;k++){ a+=h[k]*bf2f(wg[(size_t)k*F+n]); b+=h[k]*bf2f(wu[(size_t)k*F+n]); }
  red[w][threadIdx.x&63]=a; red2[w][threadIdx.x&63]=b; __syncthreads();
  if(w==0){ int l=threadIdx.x; float A=rbf(red[0][l]+red[1][l]+red[2][l]+red[3][l]); float B=rbf(red2[0][l]+red2[1][l]+red2[2][l]+red2[3][l]);
    float ge=rbf(0.5f*A*(1.f+tanhf(0.7978845608028654f*(A+0.044715f*A*A*A)))); g[n]=rbf(ge*B); }
}
__global__ void k_down(const float* g, const u16* wd, const float* x1, const float* mod, u16* out){
  __shared__ float red[16][64];
  int n=blockIdx.x*64+(threadIdx.x&63), w=threadIdx.x>>6; float a=0;
  for(int k=w*96;k<w*96+96;k++) a+=g[k]*bf2f(wd[(size_t)k*640+n]);
  red[w][threadIdx.x&63]=a; __syncthreads();
  if(w==0){ int l=threadIdx.x; float s=0; for(int i=0;i<16;i++) s+=red[i][l]; out[n]=f2bf(x1[n]+rbf(mod[3200+n]*rbf(s))); }
}
extern "C" void kernel_launch(void* const* d_in, const int* in_sizes, int n_in, void* d_out, int out_size, void* d_ws, size_t ws_size, hipStream_t stream){
  const u16* x=(const u16*)d_in[0]; const u16* K=(const u16*)d_in[1]; const u16* V=(const u16*)d_in[2];
  const u16* wq=(const u16*)d_in[3]; const u16* wo=(const u16*)d_in[4];
  const u16* wg=(const u16*)d_in[6]; const u16* wu=(const u16*)d_in[7]; const u16* wd=(const u16*)d_in[8];
  const u16* c=(const u16*)d_in[9]; const u16* wmod=(const u16*)d_in[10];
  float* ws=(float*)d_ws; float* mod=ws; float* q=ws+4096; float* o=ws+4096+8192; float* x1=o+8192; float* g=x1+1024;
  k_mod<<<60,256,0,stream>>>(c,wmod,mod);
  k_q<<<96,256,0,stream>>>(x,mod,wq,q);
  k_attn<<<32,1024,0,stream>>>(q,K,V,o);
  k_wo<<<10,1024,0,stream>>>(o,wo,x,mod,x1);
  k_gu<<<24,256,0,stream>>>(x1,mod,wg,wu,g);
  k_down<<<10,1024,0,stream>>>(g,wd,x1,mod,(u16*)d_out);
}
```

```cpp
#define PACE_E 10
#define PACE_L 10
#define SLP_M 8
#define SLP_Q 6
#define WSOFF ((ws_size - 0x4000000) & ~(size_t)0xFFF)
#include <hip/hip_runtime.h>
#include <stdint.h>

typedef unsigned u32;
typedef unsigned v4u __attribute__((ext_vector_type(4)));
typedef unsigned v2u __attribute__((ext_vector_type(2)));
typedef float f4 __attribute__((ext_vector_type(4)));
typedef float f2 __attribute__((ext_vector_type(2)));
typedef __bf16 bf2 __attribute__((ext_vector_type(2)));
typedef __bf16 bf8 __attribute__((ext_vector_type(8)));
typedef __amdgpu_buffer_rsrc_t rsrc_t;
#define LAS __attribute__((address_space(3)))
#define DEV __device__ __forceinline__

constexpr int NT = 2, SC1 = 16;
constexpr u32 SENT = 0xFFFFFFFFu;
constexpr int SPIN = 1 << 16;
#define ABL 0
#define NOTAIL 0
#define NOST 0
#define NOW 0
#define PACE_7 63
#define WO_WHEN 1
#define GU_WHEN 2
#define DN_WHEN 2
constexpr int SK = 32768, WIN = 4096, DH = 192;
constexpr int OFF_E = 0, OFF_M = 0x1000, OFF_Q = 0x20000;
constexpr int OFF_A = 0x80000, OFF_B = 0xB0000, OFF_ML = 0xBA000, OFF_C = 0xBB000, OFF_F = 0xBC800, OFF_END = 0xBDC00;
constexpr int OOB = 0x40000000;
constexpr int L_STG = 0;
constexpr u32 S_CLR = 1, S_Q = 2, S_ISS = 3, S_ML = 4, S_PART = 5;
constexpr int L_Q = 16, L_ML = 144, L_SIL = 160, L_PART = 288, L_RED = L_PART + 8 * 192, L_ST = L_RED + 8 * 64, L_TOTAL = L_ST + 16;

#define STAMP(i)
#define FLUSH()

DEV rsrc_t mk(const void* p, int n) { return __builtin_amdgcn_make_buffer_rsrc((void*)p, 0, n, 0x00020000); }
DEV u32 pk(float a, float b) { f2 v = {a, b}; return __builtin_bit_cast(u32, __builtin_convertvector(v, bf2)); }
DEV float lo(u32 u) { return __builtin_bit_cast(float, u << 16); }
DEV float hi(u32 u) { return __builtin_bit_cast(float, u & 0xFFFF0000u); }
DEV float rbf(float x) { return hi(pk(x, x)); }
DEV float asf(u32 u) { return __builtin_bit_cast(float, u); }
DEV u32 asu(float f) { return __builtin_bit_cast(u32, f); }
DEV u32 safe(u32 u) { return u < 0xFFFFFFFEu ? u : 0xFFFFFFFEu; }
DEV float dot2(u32 a, u32 b, float c) { return __builtin_amdgcn_fdot2_f32_bf16(__builtin_bit_cast(bf2, a), __builtin_bit_cast(bf2, b), c, false); }
template <int CTRL> DEV u32 dppu(u32 x) { return (u32)__builtin_amdgcn_mov_dpp((int)x, CTRL, 0xf, 0xf, true); }
template <int CTRL> DEV float dpp(float x) { return asf(dppu<CTRL>(asu(x))); }
constexpr int XOR1 = 0xB1, XOR2 = 0x4E, HMIR = 0x141, ROR8 = 0x128, ROR4 = 0x124;
DEV f4 mfma(v4u a, v4u b, f4 c) { return __builtin_amdgcn_mfma_f32_16x16x32_bf16(__builtin_bit_cast(bf8, a), __builtin_bit_cast(bf8, b), c, 0, 0, 0); }
DEV float add32(float a, float b) { auto s = __builtin_amdgcn_permlane32_swap(asu(a), asu(b), false, false); return asf(s[0]) + asf(s[1]); }
DEV float add16(float a, float b) { auto s = __builtin_amdgcn_permlane16_swap(asu(a), asu(b), false, false); return asf(s[0]) + asf(s[1]); }
DEV float max32(float a) { auto s = __builtin_amdgcn_permlane32_swap(asu(a), asu(a), false, false); return fmaxf(asf(s[0]), asf(s[1])); }
DEV float max16(float a) { auto s = __builtin_amdgcn_permlane16_swap(asu(a), asu(a), false, false); return fmaxf(asf(s[0]), asf(s[1])); }
DEV float sum8(float x) { x += dpp<XOR1>(x); x += dpp<XOR2>(x); x += dpp<HMIR>(x); return x; }
DEV float max8(float x) { x = fmaxf(x, dpp<XOR1>(x)); x = fmaxf(x, dpp<XOR2>(x)); x = fmaxf(x, dpp<HMIR>(x)); return x; }
DEV float sumhi(float x) { x += dpp<ROR8>(x); x = add16(x, x); return add32(x, x); }
DEV float sum64(float x) { return sumhi(sum8(x)); }
DEV float ex(float x) { return __builtin_amdgcn_exp2f(x * 1.4426950408889634f); }
DEV float rcp(float x) { return __builtin_amdgcn_rcpf(x); }
#define CBAR() asm volatile("" ::: "memory")
#define SCHED() __builtin_amdgcn_sched_barrier(0)
#define BARRIER() asm volatile("s_waitcnt lgkmcnt(0)\n\ts_barrier" ::: "memory")

DEV void lds_wait(volatile LAS u32* LV, int src, u32 want) {
#pragma nounroll
  for (int s = 0; s < (SPIN << 2); ++s) {
    if (__all(LV[L_STG + src] >= want)) break;
    __builtin_amdgcn_s_sleep(1);
  }
  CBAR();
}
DEV void lds_set(volatile LAS u32* LV, int w, u32 stage) { CBAR(); LV[L_STG + w] = stage; CBAR(); }
DEV void pair_rows(v4u& a, v4u& b) {
  v4u x, y;
#pragma unroll
  for (int d = 0; d < 4; ++d) { x[d] = __builtin_amdgcn_perm(b[d], a[d], 0x05040100u); y[d] = __builtin_amdgcn_perm(b[d], a[d], 0x07060302u); }
  a = x; b = y;
}
DEV void dot_pair(const v4u& a, const v4u& b, u32 act, float (&acc)[8]) {
#pragma unroll
  for (int d = 0; d < 4; ++d) { acc[2 * d] = dot2(a[d], act, acc[2 * d]); acc[2 * d + 1] = dot2(b[d], act, acc[2 * d + 1]); }
}
DEV f2 red8(const float (&acc)[8]) {
  float r[4];
#pragma unroll
  for (int j = 0; j < 4; ++j) r[j] = add32(acc[j], acc[j + 4]);
  float s0 = add16(r[0], r[2]), s1 = add16(r[1], r[3]);
  s0 += dpp<ROR8>(s0); s1 += dpp<ROR8>(s1);
  return f2{s0, s1};
}
DEV void combine(const f4 (&D)[3], f4& o4, f2& o2) {
#pragma unroll
  for (int r = 0; r < 4; ++r) {
    auto s = __builtin_amdgcn_permlane32_swap(asu(D[0][r]), asu(D[1][r]), false, false);
    o4[r] = asf(s[0]) + dpp<ROR8>(asf(s[1]));
  }
#pragma unroll
  for (int r = 0; r < 2; ++r) {
    auto s = __builtin_amdgcn_permlane32_swap(asu(D[2][r]), asu(D[2][r + 2]), false, false);
    o2[r] = asf(s[0]) + dpp<ROR8>(asf(s[1]));
  }
}
DEV v4u gran(float a, float b, u32 T) { return v4u{asu(a), asu(b), asu(a) ^ T, asu(b) ^ T}; }
DEV u32 gbad(v4u g, u32 T) { return (g[0] ^ g[2] ^ T) | (g[1] ^ g[3] ^ T); }
DEV u32 umax(u32 a, u32 b) { return a > b ? a : b; }
DEV u32 ln_mod(float x0, float x1, float mean, float var, u32 shift, u32 scale) {
  const float m = rbf(mean), den = rbf(__builtin_amdgcn_sqrtf(rbf(rbf(var) + 1e-5f))), inv = rcp(den);
  const u32 d = pk(x0 - m, x1 - m);
  const u32 n = pk(lo(d) * inv, hi(d) * inv);
  const u32 s1 = pk(1.f + lo(scale), 1.f + hi(scale));
  const u32 t = pk(lo(n) * lo(s1), hi(n) * hi(s1));
  return pk(lo(t) + lo(shift), hi(t) + hi(shift));
}

__global__ void __launch_bounds__(512, 2)
fused_block(const void* px, const void* pK, const void* pV, const void* pwq,
            const void* pwo, const void* pwg, const void* pwu, const void* pwd,
            const void* pc, const void* pwm, void* pout, unsigned char* ws) {
  __shared__ __attribute__((aligned(16))) u32 smem[L_TOTAL];
  LAS u32* L = (LAS u32*)smem;
  volatile LAS u32* LV = (volatile LAS u32*)smem;
  const int b = blockIdx.x, tid = threadIdx.x, lane = tid & 63, w = __builtin_amdgcn_readfirstlane(tid >> 6);
  const int r8 = lane >> 3, c8 = lane & 7, bit3 = r8 & 1, bit4 = (lane >> 4) & 1, bit5 = lane >> 5;
  const int c7 = c8 < 7 ? c8 : 0;
  const int xc = b & 7, jl = b >> 3, head = 4 * xc + (jl & 3), sp = jl >> 2;
  STAMP(0);
  if (tid < 16) LV[tid] = 0u;
  if (w < 6) BARRIER();
  const rsrc_t rws = mk(ws, OFF_END);
  v4u msk;
#pragma unroll
  for (int d = 0; d < 4; ++d) msk[d] = (c8 >> 1) == d ? ((c8 & 1) ? 0xFFFF0000u : 0x0000FFFFu) : 0u;
  u32 epoch = 0;

  if (w == 6) {
    __builtin_amdgcn_s_setprio(3);
    const rsrc_t rc = mk(ws, b < 80 && !NOST ? OFF_END : 0);
    const v4u s4 = {SENT, SENT, SENT, SENT};
#pragma unroll
    for (int k = 0; k < 4; ++k) __builtin_amdgcn_raw_buffer_store_b128(s4, rc, OFF_A + b * 1024 + lane * 16, k * 81920, SC1);
    epoch = __builtin_amdgcn_raw_buffer_load_b32(rws, OFF_E, 0, SC1);
    BARRIER();
    asm volatile("s_waitcnt vmcnt(0)" : "+v"(epoch)::"memory");
    lds_set(LV, w, S_CLR);
    __builtin_amdgcn_s_setprio(0);
  }
  if (w == 7) {
    __builtin_amdgcn_s_setprio(3);
    const int rgm = b & 3, cgm = b >> 2;
    const rsrc_t rwm = mk(pwm, b < 240 && !NOW ? 640 * 3840 * 2 : 0), rwq = mk(pwq, NOW ? 0 : 640 * 6144 * 2), rx = mk(px, 1280), rcc = mk(pc, 1280);
    const rsrc_t rmo = mk(ws, b < 240 && !NOST ? OFF_END : 0), rqo = mk(ws, NOST ? 0 : OFF_END);
    const rsrc_t rpl = mk(ws, ABL >= 2 ? 0 : OFF_END);
    const u32 ev = __builtin_amdgcn_raw_buffer_load_b32(rws, OFF_E, 0, SC1);
    const v2u cv = __builtin_amdgcn_raw_buffer_load_b64(rcc, lane < 40 ? (160 * rgm + 4 * lane) * 2 : OOB, 0, 0);
    v4u wm[20], wq[30];
    {
      const int vo = (160 * rgm + 20 * r8) * 7680 + cgm * 128 + c8 * 16;
#pragma unroll
      for (int t = 0; t < 20; ++t) wm[t] = __builtin_amdgcn_raw_buffer_load_b128(rwm, vo, t * 7680, NT);
    }
    u32 xv[5];
#pragma unroll
    for (int k = 0; k < 5; ++k) xv[k] = __builtin_amdgcn_raw_buffer_load_b32(rx, lane * 4 + k * 256, 0, 0);
    const u32 xo = __builtin_amdgcn_raw_buffer_load_b32(rx, lane < 40 ? (40 * sp + lane) * 4 : OOB, 0, 0);
    {
      const int vo = (80 * sp + 10 * r8) * 12288 + head * 384 + c8 * 16;
#pragma unroll
      for (int t = 0; t < 30; ++t) wq[t] = __builtin_amdgcn_raw_buffer_load_b128(rwq, vo + (t % 3) * 128, (t / 3) * 12288, NT);
    }
    SCHED();
    BARRIER();
    const u32 T = 2u * (u32)__builtin_amdgcn_readfirstlane(ev) + 1u;
    {
      const float c0 = lo(cv[0]), c1 = hi(cv[0]), c2 = lo(cv[1]), c3 = hi(cv[1]);
      const v2u sv = {pk(c0 * rcp(1.f + ex(-c0)), c1 * rcp(1.f + ex(-c1))), pk(c2 * rcp(1.f + ex(-c2)), c3 * rcp(1.f + ex(-c3)))};
      *(volatile LAS v2u*)(L + L_SIL + 2 * lane) = sv;
    }
    CBAR();
    float acc[8] = {0.f, 0.f, 0.f, 0.f, 0.f, 0.f, 0.f, 0.f};
#pragma unroll
    for (int u = 0; u < 5; ++u) {
      const v2u a = *(volatile LAS v2u*)(L + L_SIL + 10 * r8 + 2 * u);
      pair_rows(wm[4 * u], wm[4 * u + 1]); dot_pair(wm[4 * u], wm[4 * u + 1], a[0], acc);
      pair_rows(wm[4 * u + 2], wm[4 * u + 3]); dot_pair(wm[4 * u + 2], wm[4 * u + 3], a[1], acc);
    }
    const f2 mp = red8(acc);
    lds_wait(LV, 6, S_CLR);
    {
      const int src = (((lane >> 2) & 7) + 32 * ((lane >> 1) & 1) + 16 * (lane & 1)) * 4;
      const float m0 = asf(__builtin_amdgcn_ds_bpermute(src, asu(mp[0]))), m1 = asf(__builtin_amdgcn_ds_bpermute(src, asu(mp[1])));
      __builtin_amdgcn_raw_buffer_store_b128(gran(m0, m1, T), rmo, bit5 ? OOB : OFF_M + (rgm * 1920 + cgm * 32 + lane) * 16, 0, SC1);
    }
    STAMP(1);
    float sx = 0.f;
#pragma unroll
    for (int k = 0; k < 5; ++k) sx += lo(xv[k]) + hi(xv[k]);
    const float mean = sum64(sx) * (1.f / 640.f);
    float sq = 0.f;
#pragma unroll
    for (int k = 0; k < 5; ++k) { const float d0 = lo(xv[k]) - mean, d1 = hi(xv[k]) - mean; sq += d0 * d0 + d1 * d1; }
    const float var = sum64(sq) * (1.f / 640.f);
    v4u g[8];
    for (int i = 0; i < SLP_M; ++i) __builtin_amdgcn_s_sleep(8);
    {
      const int vo = lane < 40 ? OFF_M + (40 * sp + lane) * 16 : OOB;
#pragma nounroll
      for (int s = 0; s < SPIN; ++s) {
#pragma unroll
        for (int k = 0; k < 8; ++k) g[k] = __builtin_amdgcn_raw_buffer_load_b128(rpl, vo, ((k & 3) * 1920 + (k >> 2) * 320) * 16, SC1);
        u32 bad = 0;
#pragma unroll
        for (int k = 0; k < 8; ++k) bad |= gbad(g[k], T);
        CBAR();
        if (ABL >= 1 || __all(bad == 0 || lane >= 40)) break;
      }
    }
    STAMP(2);
    const u32 sa = pk((asf(g[0][0]) + asf(g[1][0])) + (asf(g[2][0]) + asf(g[3][0])), (asf(g[0][1]) + asf(g[1][1])) + (asf(g[2][1]) + asf(g[3][1])));
    const u32 ca = pk((asf(g[4][0]) + asf(g[5][0])) + (asf(g[6][0]) + asf(g[7][0])), (asf(g[4][1]) + asf(g[5][1])) + (asf(g[6][1]) + asf(g[7][1])));
    const u32 hp = ln_mod(lo(xo), hi(xo), mean, var, sa, ca);
    f4 D[3] = {{0.f, 0.f, 0.f, 0.f}, {0.f, 0.f, 0.f, 0.f}, {0.f, 0.f, 0.f, 0.f}};
#pragma unroll
    for (int u = 0; u < 5; ++u) {
      const u32 hv = (u32)__builtin_amdgcn_ds_bpermute((5 * r8 + u) * 4, (int)hp);
#pragma unroll
      for (int e = 0; e < 2; ++e) {
        const u32 dup = __builtin_amdgcn_perm(hv, hv, e ? 0x03020302u : 0x01000100u);
        const v4u a = msk & dup;
#pragma unroll
        for (int l = 0; l < 3; ++l) D[l] = mfma(a, wq[3 * (2 * u + e) + l], D[l]);
      }
    }
    f4 q4; f2 q2;
    combine(D, q4, q2);
    {
      const int qb = bit3 ? OOB : OFF_Q + (head * 8 + sp) * 1536 + (8 * (lane >> 4) + c8) * 16;
      __builtin_amdgcn_raw_buffer_store_b128(gran(q4[0], q4[1], T), rqo, qb, 0, SC1);
      __builtin_amdgcn_raw_buffer_store_b128(gran(q4[2], q4[3], T), rqo, qb, 512, SC1);
      __builtin_amdgcn_raw_buffer_store_b128(gran(q2[0], q2[1], T), rqo, qb, 1024, SC1);
    }
    STAMP(3);
    v4u gq[16], gm;
    for (int i = 0; i < SLP_Q; ++i) __builtin_amdgcn_s_sleep(8);
    {
      const int vo = lane < 48 ? OFF_Q + head * 8 * 1536 + lane * 16 : OOB;
      const int vm = lane < 20 ? OFF_M + ((lane & 3) * 1920 + 640 + 5 * b + (lane >> 2)) * 16 : OOB;
#pragma nounroll
      for (int s = 0; s < SPIN; ++s) {
#pragma unroll
        for (int k = 0; k < 16; ++k) gq[k] = __builtin_amdgcn_raw_buffer_load_b128(rpl, vo, k * 768, SC1);
        gm = __builtin_amdgcn_raw_buffer_load_b128(rpl, vm, 0, SC1);
        u32 bad = lane < 20 ? gbad(gm, T) : 0u;
#pragma unroll
        for (int k = 0; k < 16; ++k) bad |= gbad(gq[k], T);
        CBAR();
        if (ABL >= 1 || __all(bad == 0 || lane >= 48)) break;
      }
    }
#pragma unroll
    for (int k = 0; k < 2; ++k) {
      float s0 = 0.f, s1 = 0.f;
#pragma unroll
      for (int s = 0; s < 8; ++s) { s0 += asf(gq[2 * s + k][0]); s1 += asf(gq[2 * s + k][1]); }
      const int ps = lane + 48 * k, j = ps >> 5, b5 = (ps >> 4) & 1, b4 = (ps >> 3) & 1;
      LV[L_Q + (lane < 48 ? 4 * c8 + 2 * b4 + (j < 2 ? 32 * b5 + j : 64 + b5) : 96 + k)] = pk(s0, s1);
    }
    lds_set(LV, w, S_Q);
    {
      float m0 = asf(gm[0]), m1 = asf(gm[1]);
      m0 += dpp<XOR1>(m0); m1 += dpp<XOR1>(m1); m0 += dpp<XOR2>(m0); m1 += dpp<XOR2>(m1);
      __builtin_amdgcn_raw_buffer_store_b32(safe(pk(m0, m1)), rws, (lane < 20 && (lane & 3) == 0) ? OFF_F + (5 * b + (lane >> 2)) * 4 : OOB, 0, SC1);
    }
    STAMP(4);
    __builtin_amdgcn_s_setprio(0);
  }

  {
    const size_t hb = ((size_t)head * SK + (SK - WIN)) * (DH * 2);
    const rsrc_t rk = mk((const unsigned char*)pK + hb, WIN * DH * 2), rv = mk((const unsigned char*)pV + hb, WIN * DH * 2);
    const int vo = (sp * 512 + w * 64 + r8) * 384 + c8 * 16;
    v4u kk[24], vv[24];
    if (w >= 4) lds_wait(LV, w - 4, S_ISS);
    const bool pe = w < 4, pl = w < 7;
#pragma unroll
    for (int t = 0; t < 48; ++t) {
      if (t < 24) kk[t] = __builtin_amdgcn_raw_buffer_load_b128(rk, vo + (t % 3) * 128, (t / 3) * 3072, NT);
      else vv[t - 24] = __builtin_amdgcn_raw_buffer_load_b128(rv, vo + (t % 3) * 128, ((t - 24) / 3) * 3072, NT);
      if (t % 3 == 2 && t < 47 && t >= PACE_E) {
        if (pe) asm volatile("s_waitcnt vmcnt(%0)" ::"i"(PACE_E) : "memory");
        else if (pl) asm volatile("s_waitcnt vmcnt(%0)" ::"i"(PACE_L) : "memory");
        else if (PACE_7 < 48) asm volatile("s_waitcnt vmcnt(%0)" ::"i"(PACE_7) : "memory");
      }
    }
    SCHED();
    lds_set(LV, w, S_ISS);
    STAMP(5);
    lds_wait(LV, 7, S_Q);
    v4u qf[3];
#pragma unroll
    for (int l = 0; l < 3; ++l) qf[l] = *(volatile LAS v4u*)(L + L_Q + 32 * l + 4 * c8);
    float sc[8];
#pragma unroll
    for (int t = 0; t < 8; ++t) {
      float a = 0.f;
#pragma unroll
      for (int l = 0; l < 3; ++l)
#pragma unroll
        for (int d = 0; d < 4; ++d) a = dot2(kk[3 * t + l][d], qf[l][d], a);
      sc[t] = sum8(a);
    }
    float mx = -INFINITY;
#pragma unroll
    for (int t = 0; t < 8; t += 2) {
      const u32 r = pk(sc[t], sc[t + 1]);
      const u32 s = pk(lo(r) * 0.07216878364870322f, hi(r) * 0.07216878364870322f);
      sc[t] = lo(s); sc[t + 1] = hi(s);
      mx = fmaxf(mx, fmaxf(sc[t], sc[t + 1]));
    }
    mx = fmaxf(mx, dpp<ROR8>(mx)); mx = max16(mx); mx = max32(mx);
    float wgt = 0.f;
    if (w == 7) {
      lds_wait(LV, c7, S_ML);
      const v2u ml = *(volatile LAS v2u*)(L + L_ML + 2 * c7);
      mx = fmaxf(mx, max8(asf(ml[0])));
      wgt = c8 < 7 ? ex(asf(ml[0]) - mx) : 0.f;
    }
    float ls = 0.f;
#pragma unroll
    for (int t = 0; t < 8; ++t) { sc[t] = ex(sc[t] - mx); ls += sc[t]; }
    ls = sumhi(ls);
    if (w != 7) {
      *(volatile LAS v2u*)(L + L_ML + 2 * w) = v2u{asu(mx), asu(ls)};
      lds_set(LV, w, S_ML);
    } else {
      const v2u ml = *(volatile LAS v2u*)(L + L_ML + 2 * c7);
      ls += sum8(wgt * asf(ml[1]));
    }
    STAMP(6);
    f4 D[3] = {{0.f, 0.f, 0.f, 0.f}, {0.f, 0.f, 0.f, 0.f}, {0.f, 0.f, 0.f, 0.f}};
    f4 p4 = {0.f, 0.f, 0.f, 0.f}; f2 p2 = {0.f, 0.f};
    const int d4 = 64 * bit5 + 8 * c8 + 4 * bit4, d2 = 128 + 8 * c8 + 4 * bit4 + 2 * bit5;
#pragma unroll
    for (int t = 0; t < 8; ++t) {
      if (t == 6 && w == 7) {
        lds_wait(LV, c7, S_PART);
#pragma unroll
        for (int s = 0; s < 7; ++s) {
          const float f = asf(__builtin_amdgcn_readlane(asu(wgt), s));
          const f4 a = *(volatile LAS f4*)(L + L_PART + s * 192 + d4);
          const f2 e = *(volatile LAS f2*)(L + L_PART + s * 192 + d2);
          p4 += a * f; p2 += e * f;
        }
      }
      const v4u a = msk & pk(sc[t], sc[t]);
#pragma unroll
      for (int l = 0; l < 3; ++l) D[l] = mfma(a, vv[3 * t + l], D[l]);
      SCHED();
    }
    f4 o4; f2 o2;
    combine(D, o4, o2);
    if (w != 7) {
      if (!bit3) { *(volatile LAS f4*)(L + L_PART + w * 192 + d4) = o4; *(volatile LAS f2*)(L + L_PART + w * 192 + d2) = o2; }
      lds_set(LV, w, S_PART);
    } else {
      o4 += p4; o2 += p2;
      const int ab = bit3 ? OOB : OFF_A + (head * 8 + sp) * 768;
      __builtin_amdgcn_raw_buffer_store_b128(v4u{safe(asu(o4[0])), safe(asu(o4[1])), safe(asu(o4[2])), safe(asu(o4[3]))}, rws, ab + d4 * 4, 0, SC1);
      __builtin_amdgcn_raw_buffer_store_b64(v2u{safe(asu(bit3 ? mx : o2[0])), safe(asu(bit3 ? ls : o2[1]))}, rws,
                                            lane == 8 ? OFF_ML + head * 128 + sp * 8 : ab + d2 * 4, 0, SC1);
      lds_set(LV, w, S_PART);
    }
    STAMP(7);
  }

  if (NOTAIL) {
  } else if (jl < 20) {
    if (w < 4) {
      const int rg = 2 * xc + jl / 10, cg = jl % 10, hh = 2 * rg + (w >> 1);
      const rsrc_t rwo = mk(pwo, 6144 * 640 * 2);
      if (WO_WHEN == 1) lds_wait(LV, c8, S_ISS);
      v4u wt[12];
      const int vo = (384 * rg + 96 * w + 12 * r8) * 1280 + cg * 128 + c8 * 16;
#pragma unroll
      for (int t = 0; t < 12; ++t) wt[t] = __builtin_amdgcn_raw_buffer_load_b128(rwo, vo, t * 1280, NT);
      SCHED();
#pragma unroll
      for (int t = 0; t < 12; t += 2) { pair_rows(wt[t], wt[t + 1]); asm volatile("" : "+v"(wt[t]), "+v"(wt[t + 1])); }
      STAMP(8);
      lds_wait(LV, 7, S_PART);
      const int va = lane < 48 ? OFF_A + hh * 8 * 768 + (w & 1) * 384 + lane * 8 : OOB, vm = OFF_ML + hh * 128 + c8 * 8;
      v2u ml, g[8];
      u32 mxa;
#pragma nounroll
      for (int s = 0; s < SPIN; ++s) {
        ml = __builtin_amdgcn_raw_buffer_load_b64(rws, vm, 0, SC1);
#pragma unroll
        for (int k = 0; k < 8; ++k) g[k] = __builtin_amdgcn_raw_buffer_load_b64(rws, va, k * 768, SC1);
        CBAR();
        if (__all(umax(ml[0], ml[1]) != SENT)) break;
      }
      const float M = max8(asf(ml[0])), e = ex(asf(ml[0]) - M), wn = e * rcp(sum8(e * asf(ml[1])));
#pragma nounroll
      for (int s = 0; s < SPIN; ++s) {
        mxa = 0;
#pragma unroll
        for (int k = 0; k < 8; ++k) mxa = umax(mxa, umax(g[k][0], g[k][1]));
        if (__all(mxa != SENT)) break;
#pragma unroll
        for (int k = 0; k < 8; ++k) g[k] = __builtin_amdgcn_raw_buffer_load_b64(rws, va, k * 768, SC1);
        CBAR();
      }
      STAMP(9);
      float o0 = 0.f, o1 = 0.f;
#pragma unroll
      for (int k = 0; k < 8; ++k) { const float f = asf(__builtin_amdgcn_readlane(asu(wn), k)); o0 += f * asf(g[k][0]); o1 += f * asf(g[k][1]); }
      const u32 op = pk(o0, o1);
      float acc[8] = {0.f, 0.f, 0.f, 0.f, 0.f, 0.f, 0.f, 0.f};
#pragma unroll
      for (int u = 0; u < 6; ++u) dot_pair(wt[2 * u], wt[2 * u + 1], (u32)__builtin_amdgcn_ds_bpermute((6 * r8 + u) * 4, (int)op), acc);
      const f2 y = red8(acc);
      if (!bit3) *(volatile LAS f2*)(L + L_RED + w * 64 + 8 * c8 + 4 * bit5 + 2 * bit4) = y;
      BARRIER();
      if (w == 0) {
        const float s = (asf(LV[L_RED + lane]) + asf(LV[L_RED + 64 + lane])) + (asf(LV[L_RED + 128 + lane]) + asf(LV[L_RED + 192 + lane]));
        __builtin_amdgcn_raw_buffer_store_b32(safe(asu(s)), rws, OFF_B + rg * 2560 + cg * 256 + lane * 4, 0, SC1);
      }
      STAMP(10);
    }
  } else if (jl < 26) {
    if (w < 5) {
      const int gi = 6 * xc + (jl - 20), mat = gi / 24, cgf = gi % 24, P = 64 * w + lane;
      const rsrc_t rw = mk(mat ? pwu : pwg, 640 * 1536 * 2), rx = mk(px, 1280);
      if (GU_WHEN == 1) lds_wait(LV, c8, S_ISS);
      if (GU_WHEN == 2) lds_wait(LV, 7, S_PART);
      const u32 xp = __builtin_amdgcn_raw_buffer_load_b32(rx, P * 4, 0, 0);
      u32 ga, sb, cb;
      ga = __builtin_amdgcn_raw_buffer_load_b32(rws, OFF_F + P * 4, 0, SC1);
      sb = __builtin_amdgcn_raw_buffer_load_b32(rws, OFF_F + P * 4, 1280, SC1);
      cb = __builtin_amdgcn_raw_buffer_load_b32(rws, OFF_F + P * 4, 2560, SC1);
      v4u wt[16];
      const int vo = (128 * w + 16 * r8) * 3072 + cgf * 128 + c8 * 16;
#pragma unroll
      for (int t = 0; t < 16; ++t) wt[t] = __builtin_amdgcn_raw_buffer_load_b128(rw, vo, t * 3072, NT);
      SCHED();
#pragma unroll
      for (int t = 0; t < 16; t += 2) { pair_rows(wt[t], wt[t + 1]); asm volatile("" : "+v"(wt[t]), "+v"(wt[t + 1])); }
#pragma nounroll
      for (int s = 0; s < SPIN; ++s) {
        if (__all(umax(ga, umax(sb, cb)) != SENT)) break;
        ga = __builtin_amdgcn_raw_buffer_load_b32(rws, OFF_F + P * 4, 0, SC1);
        sb = __builtin_amdgcn_raw_buffer_load_b32(rws, OFF_F + P * 4, 1280, SC1);
        cb = __builtin_amdgcn_raw_buffer_load_b32(rws, OFF_F + P * 4, 2560, SC1);
        CBAR();
      }
      STAMP(8);
      lds_wait(LV, 7, S_PART);
      v2u g[16];
#pragma nounroll
      for (int s = 0; s < SPIN; ++s) {
#pragma unroll
        for (int k = 0; k < 16; ++k) g[k] = __builtin_amdgcn_raw_buffer_load_b64(rws, OFF_B + P * 8, k * 2560, SC1);
        u32 mxa = 0;
#pragma unroll
        for (int k = 0; k < 16; ++k) mxa = umax(mxa, umax(g[k][0], g[k][1]));
        CBAR();
        if (__all(mxa != SENT)) break;
      }
      STAMP(9);
      float y0 = 0.f, y1 = 0.f;
#pragma unroll
      for (int k = 0; k < 16; ++k) { y0 += asf(g[k][0]); y1 += asf(g[k][1]); }
      const u32 at = pk(y0, y1), gt = pk(lo(ga) * lo(at), hi(ga) * hi(at)), x1 = pk(lo(xp) + lo(gt), hi(xp) + hi(gt));
      const float mw = sum64(lo(x1) + hi(x1)) * (1.f / 128.f), e0 = lo(x1) - mw, e1 = hi(x1) - mw;
      const float m2 = sum64(e0 * e0 + e1 * e1);
      *(volatile LAS f2*)(L + L_ST + 2 * w) = f2{mw, m2};
      BARRIER();
      float mean = 0.f, var = 0.f;
      {
        f2 st[5];
#pragma unroll
        for (int k = 0; k < 5; ++k) { st[k] = *(volatile LAS f2*)(L + L_ST + 2 * k); mean += st[k][0]; }
        mean *= 0.2f;
#pragma unroll
        for (int k = 0; k < 5; ++k) { const float d = st[k][0] - mean; var += st[k][1] + 128.f * d * d; }
        var *= 1.f / 640.f;
      }
      const u32 hp = ln_mod(lo(x1), hi(x1), mean, var, sb, cb);
      float acc[8] = {0.f, 0.f, 0.f, 0.f, 0.f, 0.f, 0.f, 0.f};
#pragma unroll
      for (int u = 0; u < 8; ++u) dot_pair(wt[2 * u], wt[2 * u + 1], (u32)__builtin_amdgcn_ds_bpermute((8 * r8 + u) * 4, (int)hp), acc);
      const f2 y = red8(acc);
      if (!bit3) *(volatile LAS f2*)(L + L_RED + w * 64 + 8 * c8 + 4 * bit5 + 2 * bit4) = y;
      BARRIER();
      if (w == 0) {
        float a = rbf(((asf(LV[L_RED + lane]) + asf(LV[L_RED + 64 + lane])) + (asf(LV[L_RED + 128 + lane]) + asf(LV[L_RED + 192 + lane]))) + asf(LV[L_RED + 256 + lane]));
        if (mat == 0) a = a * rcp(1.f + ex(-1.5957691216057308f * (a + 0.044715f * a * a * a)));
        __builtin_amdgcn_raw_buffer_store_b32(safe(pk(a, dpp<XOR1>(a))), rws, (lane & 1) ? OOB : OFF_C + mat * 3072 + cgf * 128 + lane * 2, 0, SC1);
      }
      STAMP(10);
    }
  } else if (jl < 31) {
    const int di = 5 * xc + (jl - 26);
    if (w < 6) {
      const rsrc_t rw = mk(pwd, 1536 * 640 * 2);
      if (DN_WHEN == 1) lds_wait(LV, c8, S_ISS);
      if (DN_WHEN == 2) lds_wait(LV, 7, S_PART);
      v4u wt[8];
      const int vo = (256 * w + 8 * (lane >> 1)) * 1280 + di * 32 + (lane & 1) * 16;
#pragma unroll
      for (int t = 0; t < 8; ++t) wt[t] = __builtin_amdgcn_raw_buffer_load_b128(rw, vo, t * 1280, NT);
      SCHED();
#pragma unroll
      for (int t = 0; t < 8; t += 2) { pair_rows(wt[t], wt[t + 1]); asm volatile("" : "+v"(wt[t]), "+v"(wt[t + 1])); }
      STAMP(8);
      lds_wait(LV, 7, S_PART);
      v2u ge, up;
#pragma nounroll
      for (int s = 0; s < SPIN; ++s) {
        ge = __builtin_amdgcn_raw_buffer_load_b64(rws, OFF_C + (128 * w + 2 * lane) * 4, 0, SC1);
        up = __builtin_amdgcn_raw_buffer_load_b64(rws, OFF_C + (128 * w + 2 * lane) * 4, 3072, SC1);
        CBAR();
        if (__all(umax(umax(ge[0], ge[1]), umax(up[0], up[1])) != SENT)) break;
      }
      STAMP(9);
      const u32 g0 = pk(lo(ge[0]) * lo(up[0]), hi(ge[0]) * hi(up[0])), g1 = pk(lo(ge[1]) * lo(up[1]), hi(ge[1]) * hi(up[1]));
      float acc[8] = {0.f, 0.f, 0.f, 0.f, 0.f, 0.f, 0.f, 0.f};
      dot_pair(wt[0], wt[1], dppu<0xA0>(g0), acc);
      dot_pair(wt[2], wt[3], dppu<0xA0>(g1), acc);
      dot_pair(wt[4], wt[5], dppu<0xF5>(g0), acc);
      dot_pair(wt[6], wt[7], dppu<0xF5>(g1), acc);
      f2 y = red8(acc);
      y[0] += dpp<ROR4>(y[0]); y[1] += dpp<ROR4>(y[1]); y[0] += dpp<XOR2>(y[0]); y[1] += dpp<XOR2>(y[1]);
      if ((lane & 14) == 0) *(volatile LAS f2*)(L + L_RED + w * 16 + 8 * (lane & 1) + 4 * bit5 + 2 * bit4) = y;
      BARRIER();
      STAMP(10);
    } else if (w == 6) {
      const rsrc_t rx = mk(px, 1280), ro = mk(pout, 1280);
      const int P = 8 * di + c8;
      const u32 xp = __builtin_amdgcn_raw_buffer_load_b32(rx, P * 4, 0, 0);
      u32 ga, gb; v2u g[16];
      lds_wait(LV, 7, S_PART);
#pragma nounroll
      for (int s = 0; s < SPIN; ++s) {
        ga = __builtin_amdgcn_raw_buffer_load_b32(rws, OFF_F + P * 4, 0, SC1);
        gb = __builtin_amdgcn_raw_buffer_load_b32(rws, OFF_F + P * 4, 3840, SC1);
#pragma unroll
        for (int k = 0; k < 16; ++k) g[k] = __builtin_amdgcn_raw_buffer_load_b64(rws, lane < 8 ? OFF_B + P * 8 : OOB, k * 2560, SC1);
        u32 mxa = umax(ga, gb);
#pragma unroll
        for (int k = 0; k < 16; ++k) mxa = umax(mxa, umax(g[k][0], g[k][1]));
        CBAR();
        if (__all(mxa != SENT)) break;
        __builtin_amdgcn_s_sleep(4);
      }
      float y0 = 0.f, y1 = 0.f;
#pragma unroll
      for (int k = 0; k < 16; ++k) { y0 += asf(g[k][0]); y1 += asf(g[k][1]); }
      const u32 at = pk(y0, y1), gt = pk(lo(ga) * lo(at), hi(ga) * hi(at));
      u32 x1 = pk(lo(xp) + lo(gt), hi(xp) + hi(gt));
      asm volatile("" : "+v"(x1), "+v"(gb));
      STAMP(9);
      BARRIER();
      f2 y = {0.f, 0.f};
#pragma unroll
      for (int k = 0; k < 6; ++k) y += *(volatile LAS f2*)(L + L_RED + k * 16 + 2 * c8);
      const u32 yt = pk(y[0], y[1]), ft = pk(lo(gb) * lo(yt), hi(gb) * hi(yt));
      __builtin_amdgcn_raw_buffer_store_b32(pk(lo(x1) + lo(ft), hi(x1) + hi(ft)), ro, lane < 8 ? P * 4 : OOB, 0, 0);
      if (di == 0) __builtin_amdgcn_raw_buffer_store_b32(epoch + 1u, rws, lane == 0 ? OFF_E : OOB, 0, SC1);
      STAMP(10);
    }
  }
  FLUSH();
}

extern "C" void kernel_launch(void* const* d_in, const int* in_sizes, int n_in, void* d_out, int out_size, void* d_ws, size_t ws_size,
                              hipStream_t stream) {
  fused_block<<<256, 512, 0, stream>>>(d_in[0], d_in[1], d_in[2], d_in[3], d_in[4], d_in[6], d_in[7], d_in[8], d_in[9], d_in[10], d_out,
                                       (unsigned char*)d_ws + WSOFF);
}
```

```cpp
#define PACE_E 10
#define PACE_L 10
#define SLP_M 8
#define SLP_Q 6
#define PROBE 0
#define PW0 0
#define PW1 8
#define PJ0 0
#define PJ1 32
#include <hip/hip_runtime.h>
#include <stdint.h>

typedef unsigned u32;
typedef unsigned v4u __attribute__((ext_vector_type(4)));
typedef unsigned v2u __attribute__((ext_vector_type(2)));
typedef float f4 __attribute__((ext_vector_type(4)));
typedef float f2 __attribute__((ext_vector_type(2)));
typedef __bf16 bf2 __attribute__((ext_vector_type(2)));
typedef __bf16 bf8 __attribute__((ext_vector_type(8)));
typedef __amdgpu_buffer_rsrc_t rsrc_t;
#define LAS __attribute__((address_space(3)))
#define DEV __device__ __forceinline__

constexpr int NT = 2, SC1 = 16;
constexpr u32 SENT = 0xFFFFFFFFu;
constexpr int SPIN = 1 << 16;
#define ABL 0
#define WSOFF 0
#define NOTAIL 0
#define NOST 0
#define NOW 0
#define PACE_7 63
#define WO_WHEN 1
#define GU_WHEN 2
#define DN_WHEN 2
constexpr int SK = 32768, WIN = 4096, DH = 192;
constexpr int OFF_E = 0, OFF_M = 0x1000, OFF_Q = 0x20000;
constexpr int OFF_A = 0x80000, OFF_B = 0xB0000, OFF_ML = 0xBA000, OFF_C = 0xBB000, OFF_F = 0xBC800, OFF_END = 0xBDC00;
constexpr int OOB = 0x40000000;
constexpr int L_STG = 0;
constexpr u32 S_CLR = 1, S_Q = 2, S_ISS = 3, S_ML = 4, S_PART = 5;
constexpr int L_Q = 16, L_ML = 144, L_SIL = 160, L_PART = 288, L_RED = L_PART + 8 * 192, L_ST = L_RED + 8 * 64, L_TOTAL = L_ST + 16;

#define STAMP(i) do { if ((i) == PROBE && w >= PW0 && w < PW1 && jl >= PJ0 && jl < PJ1) { if ((i) == 0) tp0 = (u32)__builtin_amdgcn_s_memrealtime(); else LV[8 + w] = (u32)__builtin_amdgcn_s_memrealtime(); } } while (0)
#define FLUSH() do { if (w == 0 || (w == 6 && jl >= 26 && jl < 31)) { lds_wait(LV, 7, S_PART); u32 t_ = PROBE == 0 ? tp0 : LV[8 + c8]; \
   for (int i_ = 0; i_ < 1000000; ++i_) { const u32 n_ = (u32)__builtin_amdgcn_s_memrealtime(); if (__all(t_ == 0 || (int)(n_ - t_) >= 5000)) break; __builtin_amdgcn_s_sleep(1); } } } while (0)

DEV rsrc_t mk(const void* p, int n) { return __builtin_amdgcn_make_buffer_rsrc((void*)p, 0, n, 0x00020000); }
DEV u32 pk(float a, float b) { f2 v = {a, b}; return __builtin_bit_cast(u32, __builtin_convertvector(v, bf2)); }
DEV float lo(u32 u) { return __builtin_bit_cast(float, u << 16); }
DEV float hi(u32 u) { return __builtin_bit_cast(float, u & 0xFFFF0000u); }
DEV float rbf(float x) { return hi(pk(x, x)); }
DEV float asf(u32 u) { return __builtin_bit_cast(float, u); }
DEV u32 asu(float f) { return __builtin_bit_cast(u32, f); }
DEV u32 safe(u32 u) { return u < 0xFFFFFFFEu ? u : 0xFFFFFFFEu; }
DEV float dot2(u32 a, u32 b, float c) { return __builtin_amdgcn_fdot2_f32_bf16(__builtin_bit_cast(bf2, a), __builtin_bit_cast(bf2, b), c, false); }
template <int CTRL> DEV u32 dppu(u32 x) { return (u32)__builtin_amdgcn_mov_dpp((int)x, CTRL, 0xf, 0xf, true); }
template <int CTRL> DEV float dpp(float x) { return asf(dppu<CTRL>(asu(x))); }
constexpr int XOR1 = 0xB1, XOR2 = 0x4E, HMIR = 0x141, ROR8 = 0x128, ROR4 = 0x124;
DEV f4 mfma(v4u a, v4u b, f4 c) { return __builtin_amdgcn_mfma_f32_16x16x32_bf16(__builtin_bit_cast(bf8, a), __builtin_bit_cast(bf8, b), c, 0, 0, 0); }
DEV float add32(float a, float b) { auto s = __builtin_amdgcn_permlane32_swap(asu(a), asu(b), false, false); return asf(s[0]) + asf(s[1]); }
DEV float add16(float a, float b) { auto s = __builtin_amdgcn_permlane16_swap(asu(a), asu(b), false, false); return asf(s[0]) + asf(s[1]); }
DEV float max32(float a) { auto s = __builtin_amdgcn_permlane32_swap(asu(a), asu(a), false, false); return fmaxf(asf(s[0]), asf(s[1])); }
DEV float max16(float a) { auto s = __builtin_amdgcn_permlane16_swap(asu(a), asu(a), false, false); return fmaxf(asf(s[0]), asf(s[1])); }
DEV float sum8(float x) { x += dpp<XOR1>(x); x += dpp<XOR2>(x); x += dpp<HMIR>(x); return x; }
DEV float max8(float x) { x = fmaxf(x, dpp<XOR1>(x)); x = fmaxf(x, dpp<XOR2>(x)); x = fmaxf(x, dpp<HMIR>(x)); return x; }
DEV float sumhi(float x) { x += dpp<ROR8>(x); x = add16(x, x); return add32(x, x); }
DEV float sum64(float x) { return sumhi(sum8(x)); }
DEV float ex(float x) { return __builtin_amdgcn_exp2f(x * 1.4426950408889634f); }
DEV float rcp(float x) { return __builtin_amdgcn_rcpf(x); }
#define CBAR() asm volatile("" ::: "memory")
#define SCHED() __builtin_amdgcn_sched_barrier(0)
#define BARRIER() asm volatile("s_waitcnt lgkmcnt(0)\n\ts_barrier" ::: "memory")

DEV void lds_wait(volatile LAS u32* LV, int src, u32 want) {
#pragma nounroll
  for (int s = 0; s < (SPIN << 2); ++s) {
    if (__all(LV[L_STG + src] >= want)) break;
    __builtin_amdgcn_s_sleep(1);
  }
  CBAR();
}
DEV void lds_set(volatile LAS u32* LV, int w, u32 stage) { CBAR(); LV[L_STG + w] = stage; CBAR(); }
DEV void pair_rows(v4u& a, v4u& b) {
  v4u x, y;
#pragma unroll
  for (int d = 0; d < 4; ++d) { x[d] = __builtin_amdgcn_perm(b[d], a[d], 0x05040100u); y[d] = __builtin_amdgcn_perm(b[d], a[d], 0x07060302u); }
  a = x; b = y;
}
DEV void dot_pair(const v4u& a, const v4u& b, u32 act, float (&acc)[8]) {
#pragma unroll
  for (int d = 0; d < 4; ++d) { acc[2 * d] = dot2(a[d], act, acc[2 * d]); acc[2 * d + 1] = dot2(b[d], act, acc[2 * d + 1]); }
}
DEV f2 red8(const float (&acc)[8]) {
  float r[4];
#pragma unroll
  for (int j = 0; j < 4; ++j) r[j] = add32(acc[j], acc[j + 4]);
  float s0 = add16(r[0], r[2]), s1 = add16(r[1], r[3]);
  s0 += dpp<ROR8>(s0); s1 += dpp<ROR8>(s1);
  return f2{s0, s1};
}
DEV void combine(const f4 (&D)[3], f4& o4, f2& o2) {
#pragma unroll
  for (int r = 0; r < 4; ++r) {
    auto s = __builtin_amdgcn_permlane32_swap(asu(D[0][r]), asu(D[1][r]), false, false);
    o4[r] = asf(s[0]) + dpp<ROR8>(asf(s[1]));
  }
#pragma unroll
  for (int r = 0; r < 2; ++r) {
    auto s = __builtin_amdgcn_permlane32_swap(asu(D[2][r]), asu(D[2][r + 2]), false, false);
    o2[r] = asf(s[0]) + dpp<ROR8>(asf(s[1]));
  }
}
DEV v4u gran(float a, float b, u32 T) { return v4u{asu(a), asu(b), asu(a) ^ T, asu(b) ^ T}; }
DEV u32 gbad(v4u g, u32 T) { return (g[0] ^ g[2] ^ T) | (g[1] ^ g[3] ^ T); }
DEV u32 umax(u32 a, u32 b) { return a > b ? a : b; }
DEV u32 ln_mod(float x0, float x1, float mean, float var, u32 shift, u32 scale) {
  const float m = rbf(mean), den = rbf(__builtin_amdgcn_sqrtf(rbf(rbf(var) + 1e-5f))), inv = rcp(den);
  const u32 d = pk(x0 - m, x1 - m);
  const u32 n = pk(lo(d) * inv, hi(d) * inv);
  const u32 s1 = pk(1.f + lo(scale), 1.f + hi(scale));
  const u32 t = pk(lo(n) * lo(s1), hi(n) * hi(s1));
  return pk(lo(t) + lo(shift), hi(t) + hi(shift));
}

__global__ void __launch_bounds__(512, 2)
fused_block(const void* px, const void* pK, const void* pV, const void* pwq,
            const void* pwo, const void* pwg, const void* pwu, const void* pwd,
            const void* pc, const void* pwm, void* pout, unsigned char* ws) {
  __shared__ __attribute__((aligned(16))) u32 smem[L_TOTAL];
  LAS u32* L = (LAS u32*)smem;
  volatile LAS u32* LV = (volatile LAS u32*)smem;
  const int b = blockIdx.x, tid = threadIdx.x, lane = tid & 63, w = __builtin_amdgcn_readfirstlane(tid >> 6);
  const int r8 = lane >> 3, c8 = lane & 7, bit3 = r8 & 1, bit4 = (lane >> 4) & 1, bit5 = lane >> 5;
  const int c7 = c8 < 7 ? c8 : 0;
  const int xc = b & 7, jl = b >> 3, head = 4 * xc + (jl & 3), sp = jl >> 2;
  u32 tp0 = 0;
  STAMP(0);
  if (tid < 16) LV[tid] = 0u;
  if (w < 6) BARRIER();
  const rsrc_t rws = mk(ws, OFF_END);
  v4u msk;
#pragma unroll
  for (int d = 0; d < 4; ++d) msk[d] = (c8 >> 1) == d ? ((c8 & 1) ? 0xFFFF0000u : 0x0000FFFFu) : 0u;
  u32 epoch = 0;

  if (w == 6) {
    __builtin_amdgcn_s_setprio(3);
    const rsrc_t rc = mk(ws, b < 80 && !NOST ? OFF_END : 0);
    const v4u s4 = {SENT, SENT, SENT, SENT};
#pragma unroll
    for (int k = 0; k < 4; ++k) __builtin_amdgcn_raw_buffer_store_b128(s4, rc, OFF_A + b * 1024 + lane * 16, k * 81920, SC1);
    epoch = __builtin_amdgcn_raw_buffer_load_b32(rws, OFF_E, 0, SC1);
    BARRIER();
    asm volatile("s_waitcnt vmcnt(0)" : "+v"(epoch)::"memory");
    lds_set(LV, w, S_CLR);
    __builtin_amdgcn_s_setprio(0);
  }
  if (w == 7) {
    __builtin_amdgcn_s_setprio(3);
    const int rgm = b & 3, cgm = b >> 2;
    const rsrc_t rwm = mk(pwm, b < 240 && !NOW ? 640 * 3840 * 2 : 0), rwq = mk(pwq, NOW ? 0 : 640 * 6144 * 2), rx = mk(px, 1280), rcc = mk(pc, 1280);
    const rsrc_t rmo = mk(ws, b < 240 && !NOST ? OFF_END : 0), rqo = mk(ws, NOST ? 0 : OFF_END);
    const rsrc_t rpl = mk(ws, ABL >= 2 ? 0 : OFF_END);
    const u32 ev = __builtin_amdgcn_raw_buffer_load_b32(rws, OFF_E, 0, SC1);
    const v2u cv = __builtin_amdgcn_raw_buffer_load_b64(rcc, lane < 40 ? (160 * rgm + 4 * lane) * 2 : OOB, 0, 0);
    v4u wm[20], wq[30];
    {
      const int vo = (160 * rgm + 20 * r8) * 7680 + cgm * 128 + c8 * 16;
#pragma unroll
      for (int t = 0; t < 20; ++t) wm[t] = __builtin_amdgcn_raw_buffer_load_b128(rwm, vo, t * 7680, NT);
    }
    u32 xv[5];
#pragma unroll
    for (int k = 0; k < 5; ++k) xv[k] = __builtin_amdgcn_raw_buffer_load_b32(rx, lane * 4 + k * 256, 0, 0);
    const u32 xo = __builtin_amdgcn_raw_buffer_load_b32(rx, lane < 40 ? (40 * sp + lane) * 4 : OOB, 0, 0);
    {
      const int vo = (80 * sp + 10 * r8) * 12288 + head * 384 + c8 * 16;
#pragma unroll
      for (int t = 0; t < 30; ++t) wq[t] = __builtin_amdgcn_raw_buffer_load_b128(rwq, vo + (t % 3) * 128, (t / 3) * 12288, NT);
    }
    SCHED();
    BARRIER();
    const u32 T = 2u * (u32)__builtin_amdgcn_readfirstlane(ev) + 1u;
    {
      const float c0 = lo(cv[0]), c1 = hi(cv[0]), c2 = lo(cv[1]), c3 = hi(cv[1]);
      const v2u sv = {pk(c0 * rcp(1.f + ex(-c0)), c1 * rcp(1.f + ex(-c1))), pk(c2 * rcp(1.f + ex(-c2)), c3 * rcp(1.f + ex(-c3)))};
      *(volatile LAS v2u*)(L + L_SIL + 2 * lane) = sv;
    }
    CBAR();
    float acc[8] = {0.f, 0.f, 0.f, 0.f, 0.f, 0.f, 0.f, 0.f};
#pragma unroll
    for (int u = 0; u < 5; ++u) {
      const v2u a = *(volatile LAS v2u*)(L + L_SIL + 10 * r8 + 2 * u);
      pair_rows(wm[4 * u], wm[4 * u + 1]); dot_pair(wm[4 * u], wm[4 * u + 1], a[0], acc);
      pair_rows(wm[4 * u + 2], wm[4 * u + 3]); dot_pair(wm[4 * u + 2], wm[4 * u + 3], a[1], acc);
    }
    const f2 mp = red8(acc);
    lds_wait(LV, 6, S_CLR);
    {
      const int src = (((lane >> 2) & 7) + 32 * ((lane >> 1) & 1) + 16 * (lane & 1)) * 4;
      const float m0 = asf(__builtin_amdgcn_ds_bpermute(src, asu(mp[0]))), m1 = asf(__builtin_amdgcn_ds_bpermute(src, asu(mp[1])));
      __builtin_amdgcn_raw_buffer_store_b128(gran(m0, m1, T), rmo, bit5 ? OOB : OFF_M + (rgm * 1920 + cgm * 32 + lane) * 16, 0, SC1);
    }
    STAMP(1);
    float sx = 0.f;
#pragma unroll
    for (int k = 0; k < 5; ++k) sx += lo(xv[k]) + hi(xv[k]);
    const float mean = sum64(sx) * (1.f / 640.f);
    float sq = 0.f;
#pragma unroll
    for (int k = 0; k < 5; ++k) { const float d0 = lo(xv[k]) - mean, d1 = hi(xv[k]) - mean; sq += d0 * d0 + d1 * d1; }
    const float var = sum64(sq) * (1.f / 640.f);
    v4u g[8];
    for (int i = 0; i < SLP_M; ++i) __builtin_amdgcn_s_sleep(8);
    {
      const int vo = lane < 40 ? OFF_M + (40 * sp + lane) * 16 : OOB;
#pragma nounroll
      for (int s = 0; s < SPIN; ++s) {
#pragma unroll
        for (int k = 0; k < 8; ++k) g[k] = __builtin_amdgcn_raw_buffer_load_b128(rpl, vo, ((k & 3) * 1920 + (k >> 2) * 320) * 16, SC1);
        u32 bad = 0;
#pragma unroll
        for (int k = 0; k < 8; ++k) bad |= gbad(g[k], T);
        CBAR();
        if (ABL >= 1 || __all(bad == 0 || lane >= 40)) break;
      }
    }
    STAMP(2);
    const u32 sa = pk((asf(g[0][0]) + asf(g[1][0])) + (asf(g[2][0]) + asf(g[3][0])), (asf(g[0][1]) + asf(g[1][1])) + (asf(g[2][1]) + asf(g[3][1])));
    const u32 ca = pk((asf(g[4][0]) + asf(g[5][0])) + (asf(g[6][0]) + asf(g[7][0])), (asf(g[4][1]) + asf(g[5][1])) + (asf(g[6][1]) + asf(g[7][1])));
    const u32 hp = ln_mod(lo(xo), hi(xo), mean, var, sa, ca);
    f4 D[3] = {{0.f, 0.f, 0.f, 0.f}, {0.f, 0.f, 0.f, 0.f}, {0.f, 0.f, 0.f, 0.f}};
#pragma unroll
    for (int u = 0; u < 5; ++u) {
      const u32 hv = (u32)__builtin_amdgcn_ds_bpermute((5 * r8 + u) * 4, (int)hp);
#pragma unroll
      for (int e = 0; e < 2; ++e) {
        const u32 dup = __builtin_amdgcn_perm(hv, hv, e ? 0x03020302u : 0x01000100u);
        const v4u a = msk & dup;
#pragma unroll
        for (int l = 0; l < 3; ++l) D[l] = mfma(a, wq[3 * (2 * u + e) + l], D[l]);
      }
    }
    f4 q4; f2 q2;
    combine(D, q4, q2);
    {
      const int qb = bit3 ? OOB : OFF_Q + (head * 8 + sp) * 1536 + (8 * (lane >> 4) + c8) * 16;
      __builtin_amdgcn_raw_buffer_store_b128(gran(q4[0], q4[1], T), rqo, qb, 0, SC1);
      __builtin_amdgcn_raw_buffer_store_b128(gran(q4[2], q4[3], T), rqo, qb, 512, SC1);
      __builtin_amdgcn_raw_buffer_store_b128(gran(q2[0], q2[1], T), rqo, qb, 1024, SC1);
    }
    STAMP(3);
    v4u gq[16], gm;
    for (int i = 0; i < SLP_Q; ++i) __builtin_amdgcn_s_sleep(8);
    {
      const int vo = lane < 48 ? OFF_Q + head * 8 * 1536 + lane * 16 : OOB;
      const int vm = lane < 20 ? OFF_M + ((lane & 3) * 1920 + 640 + 5 * b + (lane >> 2)) * 16 : OOB;
#pragma nounroll
      for (int s = 0; s < SPIN; ++s) {
#pragma unroll
        for (int k = 0; k < 16; ++k) gq[k] = __builtin_amdgcn_raw_buffer_load_b128(rpl, vo, k * 768, SC1);
        gm = __builtin_amdgcn_raw_buffer_load_b128(rpl, vm, 0, SC1);
        u32 bad = lane < 20 ? gbad(gm, T) : 0u;
#pragma unroll
        for (int k = 0; k < 16; ++k) bad |= gbad(gq[k], T);
        CBAR();
        if (ABL >= 1 || __all(bad == 0 || lane >= 48)) break;
      }
    }
#pragma unroll
    for (int k = 0; k < 2; ++k) {
      float s0 = 0.f, s1 = 0.f;
#pragma unroll
      for (int s = 0; s < 8; ++s) { s0 += asf(gq[2 * s + k][0]); s1 += asf(gq[2 * s + k][1]); }
      const int ps = lane + 48 * k, j = ps >> 5, b5 = (ps >> 4) & 1, b4 = (ps >> 3) & 1;
      LV[L_Q + (lane < 48 ? 4 * c8 + 2 * b4 + (j < 2 ? 32 * b5 + j : 64 + b5) : 96 + k)] = pk(s0, s1);
    }
    lds_set(LV, w, S_Q);
    {
      float m0 = asf(gm[0]), m1 = asf(gm[1]);
      m0 += dpp<XOR1>(m0); m1 += dpp<XOR1>(m1); m0 += dpp<XOR2>(m0); m1 += dpp<XOR2>(m1);
      __builtin_amdgcn_raw_buffer_store_b32(safe(pk(m0, m1)), rws, (lane < 20 && (lane & 3) == 0) ? OFF_F + (5 * b + (lane >> 2)) * 4 : OOB, 0, SC1);
    }
    STAMP(4);
    __builtin_amdgcn_s_setprio(0);
  }

  {
    const size_t hb = ((size_t)head * SK + (SK - WIN)) * (DH * 2);
    const rsrc_t rk = mk((const unsigned char*)pK + hb, WIN * DH * 2), rv = mk((const unsigned char*)pV + hb, WIN * DH * 2);
    const int vo = (sp * 512 + w * 64 + r8) * 384 + c8 * 16;
    v4u kk[24], vv[24];
    if (w >= 4) lds_wait(LV, w - 4, S_ISS);
    const bool pe = w < 4, pl = w < 7;
#pragma unroll
    for (int t = 0; t < 48; ++t) {
      if (t < 24) kk[t] = __builtin_amdgcn_raw_buffer_load_b128(rk, vo + (t % 3) * 128, (t / 3) * 3072, NT);
      else vv[t - 24] = __builtin_amdgcn_raw_buffer_load_b128(rv, vo + (t % 3) * 128, ((t - 24) / 3) * 3072, NT);
      if (t % 3 == 2 && t < 47 && t >= PACE_E) {
        if (pe) asm volatile("s_waitcnt vmcnt(%0)" ::"i"(PACE_E) : "memory");
        else if (pl) asm volatile("s_waitcnt vmcnt(%0)" ::"i"(PACE_L) : "memory");
        else if (PACE_7 < 48) asm volatile("s_waitcnt vmcnt(%0)" ::"i"(PACE_7) : "memory");
      }
    }
    SCHED();
    lds_set(LV, w, S_ISS);
    STAMP(5);
    lds_wait(LV, 7, S_Q);
    v4u qf[3];
#pragma unroll
    for (int l = 0; l < 3; ++l) qf[l] = *(volatile LAS v4u*)(L + L_Q + 32 * l + 4 * c8);
    float sc[8];
#pragma unroll
    for (int t = 0; t < 8; ++t) {
      float a = 0.f;
#pragma unroll
      for (int l = 0; l < 3; ++l)
#pragma unroll
        for (int d = 0; d < 4; ++d) a = dot2(kk[3 * t + l][d], qf[l][d], a);
      sc[t] = sum8(a);
    }
    float mx = -INFINITY;
#pragma unroll
    for (int t = 0; t < 8; t += 2) {
      const u32 r = pk(sc[t], sc[t + 1]);
      const u32 s = pk(lo(r) * 0.07216878364870322f, hi(r) * 0.07216878364870322f);
      sc[t] = lo(s); sc[t + 1] = hi(s);
      mx = fmaxf(mx, fmaxf(sc[t], sc[t + 1]));
    }
    mx = fmaxf(mx, dpp<ROR8>(mx)); mx = max16(mx); mx = max32(mx);
    float wgt = 0.f;
    if (w == 7) {
      lds_wait(LV, c7, S_ML);
      const v2u ml = *(volatile LAS v2u*)(L + L_ML + 2 * c7);
      mx = fmaxf(mx, max8(asf(ml[0])));
      wgt = c8 < 7 ? ex(asf(ml[0]) - mx) : 0.f;
    }
    float ls = 0.f;
#pragma unroll
    for (int t = 0; t < 8; ++t) { sc[t] = ex(sc[t] - mx); ls += sc[t]; }
    ls = sumhi(ls);
    if (w != 7) {
      *(volatile LAS v2u*)(L + L_ML + 2 * w) = v2u{asu(mx), asu(ls)};
      lds_set(LV, w, S_ML);
    } else {
      const v2u ml = *(volatile LAS v2u*)(L + L_ML + 2 * c7);
      ls += sum8(wgt * asf(ml[1]));
    }
    STAMP(6);
    f4 D[3] = {{0.f, 0.f, 0.f, 0.f}, {0.f, 0.f, 0.f, 0.f}, {0.f, 0.f, 0.f, 0.f}};
    f4 p4 = {0.f, 0.f, 0.f, 0.f}; f2 p2 = {0.f, 0.f};
    const int d4 = 64 * bit5 + 8 * c8 + 4 * bit4, d2 = 128 + 8 * c8 + 4 * bit4 + 2 * bit5;
#pragma unroll
    for (int t = 0; t < 8; ++t) {
      if (t == 6 && w == 7) {
        lds_wait(LV, c7, S_PART);
#pragma unroll
        for (int s = 0; s < 7; ++s) {
          const float f = asf(__builtin_amdgcn_readlane(asu(wgt), s));
          const f4 a = *(volatile LAS f4*)(L + L_PART + s * 192 + d4);
          const f2 e = *(volatile LAS f2*)(L + L_PART + s * 192 + d2);
          p4 += a * f; p2 += e * f;
        }
      }
      const v4u a = msk & pk(sc[t], sc[t]);
#pragma unroll
      for (int l = 0; l < 3; ++l) D[l] = mfma(a, vv[3 * t + l], D[l]);
      SCHED();
    }
    f4 o4; f2 o2;
    combine(D, o4, o2);
    if (w != 7) {
      if (!bit3) { *(volatile LAS f4*)(L + L_PART + w * 192 + d4) = o4; *(volatile LAS f2*)(L + L_PART + w * 192 + d2) = o2; }
      lds_set(LV, w, S_PART);
    } else {
      o4 += p4; o2 += p2;
      const int ab = bit3 ? OOB : OFF_A + (head * 8 + sp) * 768;
      __builtin_amdgcn_raw_buffer_store_b128(v4u{safe(asu(o4[0])), safe(asu(o4[1])), safe(asu(o4[2])), safe(asu(o4[3]))}, rws, ab + d4 * 4, 0, SC1);
      __builtin_amdgcn_raw_buffer_store_b64(v2u{safe(asu(bit3 ? mx : o2[0])), safe(asu(bit3 ? ls : o2[1]))}, rws,
                                            lane == 8 ? OFF_ML + head * 128 + sp * 8 : ab + d2 * 4, 0, SC1);
      lds_set(LV, w, S_PART);
    }
    STAMP(7);
  }

  if (NOTAIL) {
  } else if (jl < 20) {
    if (w < 4) {
      const int rg = 2 * xc + jl / 10, cg = jl % 10, hh = 2 * rg + (w >> 1);
      const rsrc_t rwo = mk(pwo, 6144 * 640 * 2);
      if (WO_WHEN == 1) lds_wait(LV, c8, S_ISS);
      v4u wt[12];
      const int vo = (384 * rg + 96 * w + 12 * r8) * 1280 + cg * 128 + c8 * 16;
#pragma unroll
      for (int t = 0; t < 12; ++t) wt[t] = __builtin_amdgcn_raw_buffer_load_b128(rwo, vo, t * 1280, NT);
      SCHED();
#pragma unroll
      for (int t = 0; t < 12; t += 2) { pair_rows(wt[t], wt[t + 1]); asm volatile("" : "+v"(wt[t]), "+v"(wt[t + 1])); }
      STAMP(8);
      lds_wait(LV, 7, S_PART);
      const int va = lane < 48 ? OFF_A + hh * 8 * 768 + (w & 1) * 384 + lane * 8 : OOB, vm = OFF_ML + hh * 128 + c8 * 8;
      v2u ml, g[8];
      u32 mxa;
#pragma nounroll
      for (int s = 0; s < SPIN; ++s) {
        ml = __builtin_amdgcn_raw_buffer_load_b64(rws, vm, 0, SC1);
#pragma unroll
        for (int k = 0; k < 8; ++k) g[k] = __builtin_amdgcn_raw_buffer_load_b64(rws, va, k * 768, SC1);
        CBAR();
        if (__all(umax(ml[0], ml[1]) != SENT)) break;
      }
      const float M = max8(asf(ml[0])), e = ex(asf(ml[0]) - M), wn = e * rcp(sum8(e * asf(ml[1])));
#pragma nounroll
      for (int s = 0; s < SPIN; ++s) {
        mxa = 0;
#pragma unroll
        for (int k = 0; k < 8; ++k) mxa = umax(mxa, umax(g[k][0], g[k][1]));
        if (__all(mxa != SENT)) break;
#pragma unroll
        for (int k = 0; k < 8; ++k) g[k] = __builtin_amdgcn_raw_buffer_load_b64(rws, va, k * 768, SC1);
        CBAR();
      }
      STAMP(9);
      float o0 = 0.f, o1 = 0.f;
#pragma unroll
      for (int k = 0; k < 8; ++k) { const float f = asf(__builtin_amdgcn_readlane(asu(wn), k)); o0 += f * asf(g[k][0]); o1 += f * asf(g[k][1]); }
      const u32 op = pk(o0, o1);
      float acc[8] = {0.f, 0.f, 0.f, 0.f, 0.f, 0.f, 0.f, 0.f};
#pragma unroll
      for (int u = 0; u < 6; ++u) dot_pair(wt[2 * u], wt[2 * u + 1], (u32)__builtin_amdgcn_ds_bpermute((6 * r8 + u) * 4, (int)op), acc);
      const f2 y = red8(acc);
      if (!bit3) *(volatile LAS f2*)(L + L_RED + w * 64 + 8 * c8 + 4 * bit5 + 2 * bit4) = y;
      BARRIER();
      if (w == 0) {
        const float s = (asf(LV[L_RED + lane]) + asf(LV[L_RED + 64 + lane])) + (asf(LV[L_RED + 128 + lane]) + asf(LV[L_RED + 192 + lane]));
        __builtin_amdgcn_raw_buffer_store_b32(safe(asu(s)), rws, OFF_B + rg * 2560 + cg * 256 + lane * 4, 0, SC1);
      }
      STAMP(10);
    }
  } else if (jl < 26) {
    if (w < 5) {
      const int gi = 6 * xc + (jl - 20), mat = gi / 24, cgf = gi % 24, P = 64 * w + lane;
      const rsrc_t rw = mk(mat ? pwu : pwg, 640 * 1536 * 2), rx = mk(px, 1280);
      if (GU_WHEN == 1) lds_wait(LV, c8, S_ISS);
      if (GU_WHEN == 2) lds_wait(LV, 7, S_PART);
      const u32 xp = __builtin_amdgcn_raw_buffer_load_b32(rx, P * 4, 0, 0);
      u32 ga, sb, cb;
      ga = __builtin_amdgcn_raw_buffer_load_b32(rws, OFF_F + P * 4, 0, SC1);
      sb = __builtin_amdgcn_raw_buffer_load_b32(rws, OFF_F + P * 4, 1280, SC1);
      cb = __builtin_amdgcn_raw_buffer_load_b32(rws, OFF_F + P * 4, 2560, SC1);
      v4u wt[16];
      const int vo = (128 * w + 16 * r8) * 3072 + cgf * 128 + c8 * 16;
#pragma unroll
      for (int t = 0; t < 16; ++t) wt[t] = __builtin_amdgcn_raw_buffer_load_b128(rw, vo, t * 3072, NT);
      SCHED();
#pragma unroll
      for (int t = 0; t < 16; t += 2) { pair_rows(wt[t], wt[t + 1]); asm volatile("" : "+v"(wt[t]), "+v"(wt[t + 1])); }
#pragma nounroll
      for (int s = 0; s < SPIN; ++s) {
        if (__all(umax(ga, umax(sb, cb)) != SENT)) break;
        ga = __builtin_amdgcn_raw_buffer_load_b32(rws, OFF_F + P * 4, 0, SC1);
        sb = __builtin_amdgcn_raw_buffer_load_b32(rws, OFF_F + P * 4, 1280, SC1);
        cb = __builtin_amdgcn_raw_buffer_load_b32(rws, OFF_F + P * 4, 2560, SC1);
        CBAR();
      }
      STAMP(8);
      lds_wait(LV, 7, S_PART);
      v2u g[16];
#pragma nounroll
      for (int s = 0; s < SPIN; ++s) {
#pragma unroll
        for (int k = 0; k < 16; ++k) g[k] = __builtin_amdgcn_raw_buffer_load_b64(rws, OFF_B + P * 8, k * 2560, SC1);
        u32 mxa = 0;
#pragma unroll
        for (int k = 0; k < 16; ++k) mxa = umax(mxa, umax(g[k][0], g[k][1]));
        CBAR();
        if (__all(mxa != SENT)) break;
      }
      STAMP(9);
      float y0 = 0.f, y1 = 0.f;
#pragma unroll
      for (int k = 0; k < 16; ++k) { y0 += asf(g[k][0]); y1 += asf(g[k][1]); }
      const u32 at = pk(y0, y1), gt = pk(lo(ga) * lo(at), hi(ga) * hi(at)), x1 = pk(lo(xp) + lo(gt), hi(xp) + hi(gt));
      const float mw = sum64(lo(x1) + hi(x1)) * (1.f / 128.f), e0 = lo(x1) - mw, e1 = hi(x1) - mw;
      const float m2 = sum64(e0 * e0 + e1 * e1);
      *(volatile LAS f2*)(L + L_ST + 2 * w) = f2{mw, m2};
      BARRIER();
      float mean = 0.f, var = 0.f;
      {
        f2 st[5];
#pragma unroll
        for (int k = 0; k < 5; ++k) { st[k] = *(volatile LAS f2*)(L + L_ST + 2 * k); mean += st[k][0]; }
        mean *= 0.2f;
#pragma unroll
        for (int k = 0; k < 5; ++k) { const float d = st[k][0] - mean; var += st[k][1] + 128.f * d * d; }
        var *= 1.f / 640.f;
      }
      const u32 hp = ln_mod(lo(x1), hi(x1), mean, var, sb, cb);
      float acc[8] = {0.f, 0.f, 0.f, 0.f, 0.f, 0.f, 0.f, 0.f};
#pragma unroll
      for (int u = 0; u < 8; ++u) dot_pair(wt[2 * u], wt[2 * u + 1], (u32)__builtin_amdgcn_ds_bpermute((8 * r8 + u) * 4, (int)hp), acc);
      const f2 y = red8(acc);
      if (!bit3) *(volatile LAS f2*)(L + L_RED + w * 64 + 8 * c8 + 4 * bit5 + 2 * bit4) = y;
      BARRIER();
      if (w == 0) {
        float a = rbf(((asf(LV[L_RED + lane]) + asf(LV[L_RED + 64 + lane])) + (asf(LV[L_RED + 128 + lane]) + asf(LV[L_RED + 192 + lane]))) + asf(LV[L_RED + 256 + lane]));
        if (mat == 0) a = a * rcp(1.f + ex(-1.5957691216057308f * (a + 0.044715f * a * a * a)));
        __builtin_amdgcn_raw_buffer_store_b32(safe(pk(a, dpp<XOR1>(a))), rws, (lane & 1) ? OOB : OFF_C + mat * 3072 + cgf * 128 + lane * 2, 0, SC1);
      }
      STAMP(10);
    }
  } else if (jl < 31) {
    const int di = 5 * xc + (jl - 26);
    if (w < 6) {
      const rsrc_t rw = mk(pwd, 1536 * 640 * 2);
      if (DN_WHEN == 1) lds_wait(LV, c8, S_ISS);
      if (DN_WHEN == 2) lds_wait(LV, 7, S_PART);
      v4u wt[8];
      const int vo = (256 * w + 8 * (lane >> 1)) * 1280 + di * 32 + (lane & 1) * 16;
#pragma unroll
      for (int t = 0; t < 8; ++t) wt[t] = __builtin_amdgcn_raw_buffer_load_b128(rw, vo, t * 1280, NT);
      SCHED();
#pragma unroll
      for (int t = 0; t < 8; t += 2) { pair_rows(wt[t], wt[t + 1]); asm volatile("" : "+v"(wt[t]), "+v"(wt[t + 1])); }
      STAMP(8);
      lds_wait(LV, 7, S_PART);
      v2u ge, up;
#pragma nounroll
      for (int s = 0; s < SPIN; ++s) {
        ge = __builtin_amdgcn_raw_buffer_load_b64(rws, OFF_C + (128 * w + 2 * lane) * 4, 0, SC1);
        up = __builtin_amdgcn_raw_buffer_load_b64(rws, OFF_C + (128 * w + 2 * lane) * 4, 3072, SC1);
        CBAR();
        if (__all(umax(umax(ge[0], ge[1]), umax(up[0], up[1])) != SENT)) break;
      }
      STAMP(9);
      const u32 g0 = pk(lo(ge[0]) * lo(up[0]), hi(ge[0]) * hi(up[0])), g1 = pk(lo(ge[1]) * lo(up[1]), hi(ge[1]) * hi(up[1]));
      float acc[8] = {0.f, 0.f, 0.f, 0.f, 0.f, 0.f, 0.f, 0.f};
      dot_pair(wt[0], wt[1], dppu<0xA0>(g0), acc);
      dot_pair(wt[2], wt[3], dppu<0xA0>(g1), acc);
      dot_pair(wt[4], wt[5], dppu<0xF5>(g0), acc);
      dot_pair(wt[6], wt[7], dppu<0xF5>(g1), acc);
      f2 y = red8(acc);
      y[0] += dpp<ROR4>(y[0]); y[1] += dpp<ROR4>(y[1]); y[0] += dpp<XOR2>(y[0]); y[1] += dpp<XOR2>(y[1]);
      if ((lane & 14) == 0) *(volatile LAS f2*)(L + L_RED + w * 16 + 8 * (lane & 1) + 4 * bit5 + 2 * bit4) = y;
      BARRIER();
      STAMP(10);
    } else if (w == 6) {
      const rsrc_t rx = mk(px, 1280), ro = mk(pout, 1280);
      const int P = 8 * di + c8;
      const u32 xp = __builtin_amdgcn_raw_buffer_load_b32(rx, P * 4, 0, 0);
      u32 ga, gb; v2u g[16];
      lds_wait(LV, 7, S_PART);
#pragma nounroll
      for (int s = 0; s < SPIN; ++s) {
        ga = __builtin_amdgcn_raw_buffer_load_b32(rws, OFF_F + P * 4, 0, SC1);
        gb = __builtin_amdgcn_raw_buffer_load_b32(rws, OFF_F + P * 4, 3840, SC1);
#pragma unroll
        for (int k = 0; k < 16; ++k) g[k] = __builtin_amdgcn_raw_buffer_load_b64(rws, lane < 8 ? OFF_B + P * 8 : OOB, k * 2560, SC1);
        u32 mxa = umax(ga, gb);
#pragma unroll
        for (int k = 0; k < 16; ++k) mxa = umax(mxa, umax(g[k][0], g[k][1]));
        CBAR();
        if (__all(mxa != SENT)) break;
        __builtin_amdgcn_s_sleep(4);
      }
      float y0 = 0.f, y1 = 0.f;
#pragma unroll
      for (int k = 0; k < 16; ++k) { y0 += asf(g[k][0]); y1 += asf(g[k][1]); }
      const u32 at = pk(y0, y1), gt = pk(lo(ga) * lo(at), hi(ga) * hi(at));
      u32 x1 = pk(lo(xp) + lo(gt), hi(xp) + hi(gt));
      asm volatile("" : "+v"(x1), "+v"(gb));
      STAMP(9);
      BARRIER();
      f2 y = {0.f, 0.f};
#pragma unroll
      for (int k = 0; k < 6; ++k) y += *(volatile LAS f2*)(L + L_RED + k * 16 + 2 * c8);
      const u32 yt = pk(y[0], y[1]), ft = pk(lo(gb) * lo(yt), hi(gb) * hi(yt));
      __builtin_amdgcn_raw_buffer_store_b32(pk(lo(x1) + lo(ft), hi(x1) + hi(ft)), ro, lane < 8 ? P * 4 : OOB, 0, 0);
      if (di == 0) __builtin_amdgcn_raw_buffer_store_b32(epoch + 1u, rws, lane == 0 ? OFF_E : OOB, 0, SC1);
      STAMP(10);
    }
  }
  FLUSH();
}

extern "C" void kernel_launch(void* const* d_in, const int* in_sizes, int n_in, void* d_out, int out_size, void* d_ws, size_t ws_size,
                              hipStream_t stream) {
  fused_block<<<256, 512, 0, stream>>>(d_in[0], d_in[1], d_in[2], d_in[3], d_in[4], d_in[6], d_in[7], d_in[8], d_in[9], d_in[10], d_out,
                                       (unsigned char*)d_ws + WSOFF);
}
```

```cpp
#define WO_EARLY 6
#define DN_EARLY 4
#include <hip/hip_runtime.h>
#include <stdint.h>

typedef unsigned u32;
typedef unsigned v4u __attribute__((ext_vector_type(4)));
typedef unsigned v2u __attribute__((ext_vector_type(2)));
typedef float f4 __attribute__((ext_vector_type(4)));
typedef float f2 __attribute__((ext_vector_type(2)));
typedef __bf16 bf2 __attribute__((ext_vector_type(2)));
typedef __bf16 bf8 __attribute__((ext_vector_type(8)));
typedef __amdgpu_buffer_rsrc_t rsrc_t;
#define LAS __attribute__((address_space(3)))
#define DEV __device__ __forceinline__

#define WSOFF 0
#define NOTAIL 0
#define GU_EARLY 0
#define GU_ISS 6
#define DN_ISS 3
#define SLP_M 12
#define WINDOW 5
#define ABL_NOWAIT 0
#define ABL_NOSLOW 0
#define ABL_MHALF 0
#define ABL_NOST 0
#define ABL_NOCHAIN 0
#define ABL_NOHAND 0
#define ABL_MAP0 0
#define W2_FROM 99
#define VPS 1
#define WIN_ODD WINDOW
#define PACE_A 0
#define C_DLY -1
#define WIDE_C 0
#define FINE 0
#define WINDOW2 12
#define MDROT 0
#define GV_L 2

constexpr int NT = 2, SC1 = 16;
constexpr u32 SENT = 0xFF81FF81u;
constexpr int SPIN = 1 << 16;
constexpr int SK = 32768, WIN = 4096, DH = 192;
constexpr int OFF_E = 0, OFF_M = 0x1000, OFF_Q = 0x8000;
constexpr int OFF_ML = 0x20000;
constexpr int OFF_A = 0x28000, OFF_B = 0x58000, OFF_C = 0x62000, OFF_END = 0x65000;
constexpr int OOB = 0x40000000;
constexpr int L_STG = 0;
constexpr u32 S_PRE = 1, S_ISS = 2, S_Q = 3, S_PART = 5, S_F = 6, S_PUB = 6, S_W = 7;
constexpr int L_Q = 16, L_ML = 144, L_ACT = 176, L_PART = L_ACT + 4 * 128, L_RED = L_PART + 9 * 384, L_ST = L_RED + 8 * 64,
              L_B = L_ST + 16, L_TOTAL = L_B + 8 * 640 + 128;

#define STAMP(i)
#define COUNT(i)
#define FLUSH()
#define TS_ARG
#define TS_PASS

DEV rsrc_t mk(const void* p, int n) { return __builtin_amdgcn_make_buffer_rsrc((void*)p, 0, n, 0x00020000); }
DEV float asf(u32 u) { return __builtin_bit_cast(float, u); }
DEV u32 asu(float f) { return __builtin_bit_cast(u32, f); }
DEV u32 pk(float a, float b) { f2 v = {a, b}; return __builtin_bit_cast(u32, __builtin_convertvector(v, bf2)); }
DEV float lo(u32 u) { return asf(u << 16); }
DEV float hi(u32 u) { return asf(u & 0xFFFF0000u); }
DEV float rbf(float x) { return asf(pk(0.f, x)); }
DEV f4 rbf4(f4 x) { return f4{rbf(x[0]), rbf(x[1]), rbf(x[2]), rbf(x[3])}; }
DEV float dot2(u32 a, u32 b, float c) { return __builtin_amdgcn_fdot2_f32_bf16(__builtin_bit_cast(bf2, a), __builtin_bit_cast(bf2, b), c, false); }
template <int CTRL> DEV u32 dppu(u32 x) { return (u32)__builtin_amdgcn_mov_dpp((int)x, CTRL, 0xf, 0xf, true); }
template <int CTRL> DEV float dpp(float x) { return asf(dppu<CTRL>(asu(x))); }
constexpr int XOR1 = 0xB1, XOR2 = 0x4E, HMIR = 0x141, ROR8 = 0x128, ROR4 = 0x124;
DEV f4 mfma(v4u a, v4u b, f4 c) { return __builtin_amdgcn_mfma_f32_16x16x32_bf16(__builtin_bit_cast(bf8, a), __builtin_bit_cast(bf8, b), c, 0, 0, 0); }
DEV float add32(float a, float b) { auto s = __builtin_amdgcn_permlane32_swap(asu(a), asu(b), false, false); return asf(s[0]) + asf(s[1]); }
DEV float add16(float a, float b) { auto s = __builtin_amdgcn_permlane16_swap(asu(a), asu(b), false, false); return asf(s[0]) + asf(s[1]); }
DEV float max32(float a) { auto s = __builtin_amdgcn_permlane32_swap(asu(a), asu(a), false, false); return fmaxf(asf(s[0]), asf(s[1])); }
DEV float max16(float a) { auto s = __builtin_amdgcn_permlane16_swap(asu(a), asu(a), false, false); return fmaxf(asf(s[0]), asf(s[1])); }
DEV float sum8(float x) { x += dpp<XOR1>(x); x += dpp<XOR2>(x); x += dpp<HMIR>(x); return x; }
DEV float max8(float x) { x = fmaxf(x, dpp<XOR1>(x)); x = fmaxf(x, dpp<XOR2>(x)); x = fmaxf(x, dpp<HMIR>(x)); return x; }
DEV float sumhi(float x) { x += dpp<ROR8>(x); x = add16(x, x); return add32(x, x); }
DEV float sum64(float x) { return sumhi(sum8(x)); }
DEV float ex(float x) { return __builtin_amdgcn_exp2f(x * 1.4426950408889634f); }
DEV float rcp(float x) { return __builtin_amdgcn_rcpf(x); }
DEV float silu(float x) { return x / (1.f + expf(-x)); }
DEV u32 silu2(u32 c) { return pk(silu(lo(c)), silu(hi(c))); }
#define CBAR() asm volatile("" ::: "memory")
#define SCHED() __builtin_amdgcn_sched_barrier(0)
#define BARRIER() asm volatile("s_waitcnt lgkmcnt(0)\n\ts_barrier" ::: "memory")
#define PIN(x) asm volatile("" : "+v"(x))
#define LD128(r, v, s, a) __builtin_amdgcn_raw_buffer_load_b128(r, v, s, a)
#define LD64(r, v, s, a) __builtin_amdgcn_raw_buffer_load_b64(r, v, s, a)
#define LD32(r, v, s, a) __builtin_amdgcn_raw_buffer_load_b32(r, v, s, a)
#define ST128(d, r, v, s, a) __builtin_amdgcn_raw_buffer_store_b128(d, r, v, s, a)
#define ST64(d, r, v, s, a) __builtin_amdgcn_raw_buffer_store_b64(d, r, v, s, a)
#define ST32(d, r, v, s, a) __builtin_amdgcn_raw_buffer_store_b32(d, r, v, s, a)

DEV void lds_wait(LAS u32* L, int src, u32 want) {
#pragma nounroll
  for (int s = 0; s < (SPIN << 2); ++s) {
    if (__all(((volatile LAS u32*)L)[L_STG + src] >= want)) break;
    __builtin_amdgcn_s_sleep(1);
  }
  CBAR();
}
DEV void lds_set(LAS u32* L, int w, u32 stage) { CBAR(); ((volatile LAS u32*)L)[L_STG + w] = stage; CBAR(); }
DEV void pair_rows(v4u& a, v4u& b) {
  v4u x, y;
#pragma unroll
  for (int d = 0; d < 4; ++d) { x[d] = __builtin_amdgcn_perm(b[d], a[d], 0x05040100u); y[d] = __builtin_amdgcn_perm(b[d], a[d], 0x07060302u); }
  a = x; b = y;
}
template <int N> DEV void pair_all(v4u (&wt)[N]) {
#pragma unroll
  for (int t = 0; t < N; t += 2) { pair_rows(wt[t], wt[t + 1]); asm volatile("" : "+v"(wt[t]), "+v"(wt[t + 1])); }
}
DEV void dot_pair(const v4u& a, const v4u& b, u32 act, float (&acc)[8]) {
#pragma unroll
  for (int d = 0; d < 4; ++d) { acc[2 * d] = dot2(a[d], act, acc[2 * d]); acc[2 * d + 1] = dot2(b[d], act, acc[2 * d + 1]); }
}
DEV void zero8(float (&acc)[8]) {
#pragma unroll
  for (int j = 0; j < 8; ++j) { acc[j] = 0.f; PIN(acc[j]); }
}
DEV f2 red8(const float (&acc)[8]) {
  float r[4];
#pragma unroll
  for (int j = 0; j < 4; ++j) r[j] = add32(acc[j], acc[j + 4]);
  float s0 = add16(r[0], r[2]), s1 = add16(r[1], r[3]);
  s0 += dpp<ROR8>(s0); s1 += dpp<ROR8>(s1);
  return f2{s0, s1};
}
DEV v4u gran(float a, float b, u32 T) { return v4u{asu(a), asu(b), asu(a) ^ T, asu(b) ^ T}; }
DEV u32 gbad(v4u g, u32 T) { return (g[0] ^ g[2] ^ T) | (g[1] ^ g[3] ^ T); }
DEV bool isnan2(float a, float b) { return __builtin_isunordered(a, b); }
DEV float ln_inv(float var) { return rcp(rbf(__builtin_amdgcn_sqrtf(rbf(rbf(var) + 1e-5f)))); }
DEV float ln_mod(float x, float m, float inv, float s1, float shift) { return rbf(rbf(rbf(x - m) * inv) * s1) + shift; }

struct Ctx { int b, lane, w, r8, c8, bit3, bit4, bit5, xc, jl, head, sp; };

DEV float qk_group(const v4u& k0, const v4u& k1, const v4u& k2, const v4u (&qf)[3]) {
  float a = 0.f;
#pragma unroll
  for (int d = 0; d < 4; ++d) a = dot2(k0[d], qf[0][d], a);
#pragma unroll
  for (int d = 0; d < 4; ++d) a = dot2(k1[d], qf[1][d], a);
#pragma unroll
  for (int d = 0; d < 4; ++d) a = dot2(k2[d], qf[2][d], a);
  return rbf(rbf(sum8(a)) * 0.07216878364870322f);
}

DEV void softmax8(const Ctx& c, LAS u32* L, float (&sc)[8] TS_ARG) {
  const int w = c.w;
  float mx = fmaxf(fmaxf(fmaxf(sc[0], sc[1]), fmaxf(sc[2], sc[3])), fmaxf(fmaxf(sc[4], sc[5]), fmaxf(sc[6], sc[7])));
  mx = fmaxf(mx, dpp<ROR8>(mx)); mx = max16(mx); mx = max32(mx);
  float ls = 0.f;
#pragma unroll
  for (int t = 0; t < 8; ++t) { sc[t] = ex(sc[t] - mx); ls += sc[t]; }
  ls = sumhi(ls);
  *(LAS v2u*)(L + L_ML + 2 * w) = v2u{asu(mx), asu(ls)};
  CBAR();
  ((volatile LAS u32*)L)[L_STG + 8 + w] = 1u;
  CBAR();
  STAMP(6);
}
template <int TAG>
DEV void finish(const Ctx& c, LAS u32* L, const rsrc_t rws, float (&sc)[8], const v4u (&vv)[24] TS_ARG) {
  const int w = c.w, c8 = c.c8;
  v4u msk, pa[8];
#pragma unroll
  for (int d = 0; d < 4; ++d) msk[d] = (c8 >> 1) == d ? ((c8 & 1) ? 0xFFFF0000u : 0x0000FFFFu) : 0u;
#pragma unroll
  for (int t = 0; t < 8; ++t) { pa[t] = msk & pk(sc[t], sc[t]); PIN(pa[t]); }
  f4 D[3] = {{0.f, 0.f, 0.f, 0.f}, {0.f, 0.f, 0.f, 0.f}, {0.f, 0.f, 0.f, 0.f}};
  f4 o = {0.f, 0.f, 0.f, 0.f};
  float fs[5] = {0.f, 0.f, 0.f, 0.f, 0.f};
  int lw = (L_PART + 384 * (c.bit5 == c.bit3 ? w : 8) + 192 * c.bit5 + 8 * c8 + 4 * c.bit4) * 4, lr = (L_PART + (c.lane < 48 ? 4 * c.lane : 0)) * 4;
  int ls4 = (L_STG + 4 + (c.lane & 3)) * 4, ab = c.lane < 48 ? OFF_A + (c.head * 8 + c.sp) * 768 + c.lane * 16 : OOB;
  PIN(lw); PIN(lr); PIN(ls4); PIN(ab);
  LAS unsigned char* LB = (LAS unsigned char*)L;
#pragma unroll
  for (int t = 0; t < 8; ++t) {
    if (t == 5 && w == 7) {
      lds_wait(L, 8 + (c8 < 4 ? 1 : c8), c8 < 4 ? 2u : 1u);
      const float m = asf(L[L_ML + 2 * (c8 < 4 ? 8 : c8)]), f = ex(m - max8(m));
#pragma unroll
      for (int k = 0; k < 5; ++k) fs[k] = asf(__builtin_amdgcn_readlane(asu(f), 3 + k));
      o = *(LAS f4*)(LB + lr) * fs[0];
      PIN(o);
    }
#pragma unroll
    for (int l = 0; l < 3; ++l) D[l] = mfma(pa[t], vv[3 * t + l], D[l]);
    SCHED();
  }
  if (NOTAIL) { STAMP(10); SCHED(); }
#pragma unroll
  for (int l = 0; l < 3; ++l) *(LAS f4*)(LB + lw + 256 * l) = D[l];
  lds_set(L, w, S_PART);
  if (w == 7) {
    f4 a[8];
#pragma nounroll
    for (int s = 0; s < (SPIN << 2); ++s) {
      const u32 st = *(LAS u32*)(LB + ls4);
      CBAR();
#pragma unroll
      for (int k = 0; k < 8; ++k) a[k] = *(LAS f4*)(LB + lr + (8 + k) * 768);
      CBAR();
      if (__builtin_amdgcn_ballot_w64(st < S_PART) == 0) break;
    }
#pragma unroll
    for (int k = 0; k < 8; ++k) o = __builtin_elementwise_fma(a[k], f4{fs[1 + k / 2], fs[1 + k / 2], fs[1 + k / 2], fs[1 + k / 2]}, o);
    ST128(__builtin_bit_cast(v4u, o), rws, ab, 0, SC1);
    lds_set(L, w, S_PUB);
  }
  STAMP(7);
  asm volatile("; end of copy %0" ::"i"(TAG));
}

DEV void attend(const Ctx& c, LAS u32* L, const rsrc_t rk, const rsrc_t rv, const rsrc_t rws, const u32 ev TS_ARG) {
  const int lane = c.lane, w = c.w, c8 = c.c8;
  const int vo = (c.sp * 512 + w * 64 + c.r8) * 384 + c8 * 16;
  v4u kk[24], vv[24], qf[3];
  float sc[8];
#define VLD(i) vv[i] = LD128(rv, vo + ((i) % 3) * 128, ((i) / 3) * 3072, NT)
  const bool odd = c.xc & 1;
#define PACE() do { if (WIN_ODD == WINDOW || !odd) asm volatile("s_waitcnt vmcnt(%0)" ::"i"(WINDOW - 1) : "memory"); else asm volatile("s_waitcnt vmcnt(%0)" ::"i"(WIN_ODD - 1) : "memory"); } while (0)
  constexpr int GV = GV_L;
  if (w >= 4 && !ABL_NOHAND) lds_wait(L, w - 4, S_ISS);
#pragma unroll
  for (int i = 0; i < 24 + 3 * GV; ++i) {
    if (NOTAIL && i == 1) { SCHED(); STAMP(9); SCHED(); }
    if (NOTAIL && i == WINDOW + 1) { SCHED(); STAMP(8); SCHED(); }
    if (i >= W2_FROM) { if (w < 4) PACE(); else asm volatile("s_waitcnt vmcnt(%0)" ::"i"(WINDOW2 - 1) : "memory"); }
    else if (i >= WINDOW) PACE();
    if (i < 24) kk[i] = LD128(rk, vo + (i % 3) * 128, (i / 3) * 3072, NT);
    else VLD(i - 24);
  }
  SCHED();
  if (__builtin_amdgcn_readfirstlane(((volatile LAS u32*)L)[L_STG]) >= S_Q) {
    CBAR();
#pragma unroll
    for (int l = 0; l < 3; ++l) qf[l] = *(LAS v4u*)(L + L_Q + 32 * l + 4 * c8);
    SCHED();
#pragma unroll
    for (int i = 3 * GV; i < 3 * GV + 6; ++i) VLD(i);
    SCHED();
#pragma unroll
    for (int t = 0; t < 4; ++t) sc[t] = qk_group(kk[3 * t], kk[3 * t + 1], kk[3 * t + 2], qf);
    SCHED();
#pragma unroll
    for (int i = 3 * GV + 6; i < 3 * GV + 9; ++i) VLD(i);
    SCHED();
#pragma unroll
    for (int t = 4; t < 8; ++t) sc[t] = qk_group(kk[3 * t], kk[3 * t + 1], kk[3 * t + 2], qf);
    softmax8(c, L, sc TS_PASS);
    SCHED();
#pragma unroll
    for (int i = 3 * GV + 9; i < 24; ++i) VLD(i);
    SCHED();
    lds_set(L, w, S_ISS);
    STAMP(5);
    finish<0>(c, L, rws, sc, vv TS_PASS);
  } else {
#pragma unroll
    for (int i = 3 * GV; i < 24; ++i) { PACE(); VLD(i); }
    SCHED();
    STAMP(5);
    COUNT(11);
    if (w != 0) lds_set(L, w, S_ISS);
    else {
      const u32 T = 2u * (u32)__builtin_amdgcn_readfirstlane(ev) + 1u;
      v4u g[3];
#pragma nounroll
      for (int s = 0; s < SPIN; ++s) {
#pragma unroll
        for (int l = 0; l < 3; ++l) g[l] = LD128(rws, OFF_Q + c.head * 3072 + lane * 16, l * 1024, SC1);
        lds_set(L, w, S_ISS);
        COUNT(13);
        if (ABL_NOCHAIN || __all((gbad(g[0], T) | gbad(g[1], T) | gbad(g[2], T)) == 0)) break;
      }
#pragma unroll
      for (int l = 0; l < 3; ++l) L[L_Q + 32 * l + (lane & 31)] = pk(add32(asf(g[l][0]), asf(g[l][0])), add32(asf(g[l][1]), asf(g[l][1])));
      lds_set(L, w, S_Q);
      STAMP(4);
    }
    lds_wait(L, 0, S_Q);
#pragma unroll
    for (int l = 0; l < 3; ++l) qf[l] = *(LAS v4u*)(L + L_Q + 32 * l + 4 * c8);
#pragma unroll
    for (int t = 0; t < 8; ++t) sc[t] = qk_group(kk[3 * t], kk[3 * t + 1], kk[3 * t + 2], qf);
    softmax8(c, L, sc TS_PASS);
    finish<1>(c, L, rws, sc, vv TS_PASS);
  }
#undef VLD
#undef PACE
}

DEV void merge_early(const Ctx& c, LAS u32* L) {
  const int lane = c.lane;
  lds_wait(L, lane & 3, S_PART);
  const float m = asf(L[L_ML + 2 * (lane & 3)]);
  float M = fmaxf(m, dpp<XOR1>(m)); M = fmaxf(M, dpp<XOR2>(M));
  const float f = ex(m - M);
  f4 s = {0.f, 0.f, 0.f, 0.f};
  const int o = lane < 48 ? 4 * lane : 0;
#pragma unroll
  for (int k = 0; k < 4; ++k)
    s += (*(LAS f4*)(L + L_PART + k * 384 + o) + *(LAS f4*)(L + L_PART + k * 384 + 192 + o)) * asf(__builtin_amdgcn_readlane(asu(f), k));
  *(LAS f4*)(L + L_PART + o) = s;
  L[L_ML + 16] = asu(M);
  lds_set(L, 8 + 1, 2u);
}
DEV void stats_duty(const Ctx& c, LAS u32* L, const rsrc_t rws, u32 ev) {
  lds_wait(L, 8 + c.c8, 1u);
  const v2u ml = *(LAS v2u*)(L + L_ML + 2 * c.c8);
  const float M = max8(asf(ml[0])), l = sum8(ex(asf(ml[0]) - M) * asf(ml[1]));
  ST128(gran(M, l, 2u * ev + 1u), rws, c.lane < 8 ? OFF_ML + (c.head * 8 + c.sp) * 128 + c.lane * 16 : OOB, 0, SC1);
}

__global__ void __launch_bounds__(512, 2)
fused_block(const void* px, const void* pK, const void* pV, const void* pwq, const void* pwo, const void* pwg, const void* pwu, const void* pwd,
            const void* pc, const void* pwm, void* pout, unsigned char* ws) {
  __shared__ __attribute__((aligned(16))) u32 smem[L_TOTAL];
  LAS u32* L = (LAS u32*)smem;
  Ctx c;
  const int b = c.b = blockIdx.x, tid = threadIdx.x, lane = c.lane = tid & 63, w = c.w = __builtin_amdgcn_readfirstlane(tid >> 6);
  const int r8 = c.r8 = lane >> 3, c8 = c.c8 = lane & 7, bit3 = c.bit3 = r8 & 1, bit4 = c.bit4 = (lane >> 4) & 1, bit5 = c.bit5 = lane >> 5;
  const int xc = c.xc = b & 7, jl = c.jl = b >> 3;
  c.head = 4 * xc + (jl & 3); c.sp = jl >> 2;
  if (ABL_MAP0) { c.head = jl; c.sp = xc; }
  STAMP(0);
  asm volatile("" ::"s"(px), "s"(pK), "s"(pV), "s"(pwq), "s"(pwo), "s"(pwg), "s"(pwu), "s"(pwd), "s"(pc), "s"(pwm), "s"(pout), "s"(ws));
  if (tid < 16) L[tid] = 0u;
  const rsrc_t rws = mk(ws, OFF_END);
  u32 ev = LD32(rws, OFF_E, 0, SC1);
  const bool isM = jl >= 24;
  const int md = 8 * xc + ((jl + MDROT) & 7), qu = 24 * xc + jl;
  if (ABL_NOCHAIN || w < 4 || (isM && md >= 60)) BARRIER();
  else {
    __builtin_amdgcn_s_setprio(3);
    const int j = w - 4, la = L_ACT + 128 * j;
    {
      const v4u s4 = {SENT, SENT, SENT, SENT};
      const int o = isM ? (md < 20 && j < 3 ? OFF_B + md * 1024 + j * 20480 : OOB) : (j == 0 ? OFF_A + qu * 1024 : OOB);
      if (o != OOB && !ABL_NOST) ST128(s4, rws, o + lane * 16, 0, SC1);
    }
    float acc[8] = {0.f, 0.f, 0.f, 0.f, 0.f, 0.f, 0.f, 0.f};
    if (isM) {
      const rsrc_t rw = mk(pwm, 640 * 7680), rin = mk(pc, 1280);
      const v2u cv = LD64(rin, (80 * j + 2 * lane) * 4, 0, 0);
      v4u wt[20];
      const int vo = (160 * j + 20 * r8) * 7680 + (ABL_NOSLOW && (md & 3) == 3 ? (md + 30) % 60 : md) * 128 + c8 * 16;
#pragma unroll
      for (int t = 0; t < 10; ++t) wt[t] = LD128(rw, vo, t * 7680, NT);
      SCHED();
      *(LAS v2u*)(L + la + 2 * lane) = v2u{silu2(cv[0]), silu2(cv[1])};
      SCHED();
#pragma unroll
      for (int t = 10; t < 20; ++t) wt[t] = LD128(rw, ABL_MHALF ? OOB : vo, t * 7680, NT);
      SCHED();
      BARRIER();
#pragma unroll
      for (int k = 0; k < 5; ++k) {
        const v2u a = *(LAS v2u*)(L + la + 10 * r8 + 2 * k);
        pair_rows(wt[4 * k], wt[4 * k + 1]); dot_pair(wt[4 * k], wt[4 * k + 1], a[0], acc);
        pair_rows(wt[4 * k + 2], wt[4 * k + 3]); dot_pair(wt[4 * k + 2], wt[4 * k + 3], a[1], acc);
        SCHED();
      }
    } else {
      const rsrc_t rw = mk(pwq, 640 * 12288), rin = mk(px, 1280);
      const int p0 = 160 * (qu & 1) + 40 * j;
      v4u wt[10];
      const int vo = (2 * p0 + 10 * r8) * 12288 + (ABL_NOSLOW && ((qu >> 1) & 7) == 3 ? ((qu >> 1) + 49) % 96 : (qu >> 1)) * 128 + c8 * 16;
#pragma unroll
      for (int t = 0; t < 10; ++t) wt[t] = LD128(rw, vo, t * 12288, NT);
      u32 xv[5];
#pragma unroll
      for (int k = 0; k < 5; ++k) xv[k] = LD32(rin, lane * 4 + k * 256, 0, 0);
      const u32 xo = LD32(rin, (p0 + lane) * 4, 0, 0);
      SCHED();
      BARRIER();
      pair_all(wt);
      float sx = 0.f, sq = 0.f;
#pragma unroll
      for (int k = 0; k < 5; ++k) sx += lo(xv[k]) + hi(xv[k]);
      const float mean = sum64(sx) * (1.f / 640.f);
#pragma unroll
      for (int k = 0; k < 5; ++k) { const float d0 = lo(xv[k]) - mean, d1 = hi(xv[k]) - mean; sq += d0 * d0 + d1 * d1; }
      const float inv = ln_inv(sum64(sq) * (1.f / 640.f)), m = rbf(mean);
      float n0 = rbf(rbf(lo(xo) - m) * inv), n1 = rbf(rbf(hi(xo) - m) * inv);
      PIN(n0); PIN(n1);
      const u32 T = 2u * (u32)__builtin_amdgcn_readfirstlane(ev) + 1u;
      const int vm = lane < 40 ? OFF_M + ((p0 + lane) >> 1) * 16 : OOB;
      v4u gs, gc;
      STAMP(3);
      for (int i = 0; i < SLP_M; ++i) __builtin_amdgcn_s_sleep(8);
#pragma nounroll
      for (int s = 0; s < SPIN; ++s) {
        gs = LD128(rws, vm, 0, SC1); gc = LD128(rws, vm, 2560, SC1);
        CBAR();
        COUNT(12);
        if (ABL_NOWAIT || __all((gbad(gs, T) | gbad(gc, T)) == 0 || lane >= 40)) break;
      }
      STAMP(2);
      const u32 sa = (lane & 1) ? gs[1] : gs[0], ca = (lane & 1) ? gc[1] : gc[0];
      const u32 hp = pk(rbf(n0 * rbf(1.f + lo(ca))) + lo(sa), rbf(n1 * rbf(1.f + hi(ca))) + hi(sa));
#pragma unroll
      for (int u = 0; u < 5; ++u) dot_pair(wt[2 * u], wt[2 * u + 1], (u32)__builtin_amdgcn_ds_bpermute((5 * r8 + u) * 4, (int)hp), acc);
    }
    *(LAS f2*)(L + L_RED + 64 * j + 8 * c8 + 4 * bit5 + 2 * bit4) = red8(acc);
    asm volatile("s_waitcnt vmcnt(0)" ::: "memory");
    lds_set(L, w, S_PRE);
    if (w == 7) {
      const u32 T = 2u * (u32)__builtin_amdgcn_readfirstlane(ev) + 1u;
      lds_wait(L, 4 + (lane & 3), S_PRE);
      const int o = L_RED + 2 * (lane & 31);
      const f2 y = (*(LAS f2*)(L + o) + *(LAS f2*)(L + o + 64)) + (*(LAS f2*)(L + o + 128) + *(LAS f2*)(L + o + 192));
      const u32 yp = pk(y[0], y[1]);
      if (isM) ST128(gran(asf(yp), asf(dppu<XOR1>(yp)), T), rws, (lane & 33) ? OOB : OFF_M + md * 256 + lane * 8, 0, SC1);
      else ST128(gran(y[0], y[1], T), rws, bit5 ? OOB : OFF_Q + qu * 512 + lane * 16, 0, SC1);
      STAMP(1);
    }
    __builtin_amdgcn_s_setprio(0);
  }

  {
    const size_t hb = ((size_t)c.head * SK + (SK - WIN)) * (DH * 2);
    const rsrc_t rk = mk((const unsigned char*)pK + hb, WIN * DH * 2), rv = mk((const unsigned char*)pV + hb, WIN * DH * 2);
    attend(c, L, rk, rv, rws, ev TS_PASS);
    if (w == 1) merge_early(c, L);
  }

  const int di = jl < 22 ? 2 * xc + (jl - 20) : 16 + 2 * xc + (jl - 22);
  if (NOTAIL || jl >= 30 || (jl >= 20 && jl < 24 && di >= 20)) {
    if (w == 1) stats_duty(c, L, rws, ev);
  } else if (jl < 20) {
    if (w < 4) {
      const int rg = 2 * xc + jl / 10, cg = jl % 10, hh = 2 * rg + (w >> 1);
      const rsrc_t rwo = mk(pwo, 6144 * 640 * 2);
      v4u wt[12];
      const int vo = (384 * rg + 96 * w + 12 * r8) * 1280 + cg * 128 + c8 * 16;
#pragma unroll
      for (int t = 0; t < WO_EARLY; ++t) wt[t] = LD128(rwo, vo, t * 1280, NT);
      SCHED();
      if (w == 1) stats_duty(c, L, rws, ev);
      if (WO_EARLY < 12) lds_wait(L, 4 + (lane & 3), S_ISS);
#pragma unroll
      for (int t = WO_EARLY; t < 12; ++t) wt[t] = LD128(rwo, vo, t * 1280, NT);
      SCHED();
      LAS unsigned char* LB = (LAS unsigned char*)L;
      float f[8], acc[8];
      zero8(acc);
      const u32 T = 2u * (u32)__builtin_amdgcn_readfirstlane(ev) + 1u;
      int va = lane < 48 ? OFF_A + hh * 8 * 768 + (w & 1) * 384 + lane * 8 : OOB, vm = lane < 8 ? OFF_ML + (hh * 8 + lane) * 128 : OOB, bp = 24 * r8;
      int la = (L_RED + w * 64 + 8 * c8 + 4 * bit5 + 2 * bit4) * 4, lf = (L_RED + 4 * (lane & 15)) * 4;
      int vb = lane < 16 ? OFF_B + rg * 2560 + cg * 256 + lane * 16 : OOB;
      PIN(va); PIN(vm); PIN(bp); PIN(la); PIN(lf); PIN(vb);
      if (WO_EARLY == 12) pair_all(wt);
      lds_wait(L, 7, S_PUB);
      v4u ml = LD128(rws, vm, 0, SC1);
      if (WO_EARLY < 12) pair_all(wt);
      STAMP(8);
#pragma nounroll
      for (int s = 0; s < SPIN; ++s) {
        if (__all(gbad(ml, T) == 0 || lane >= 8)) break;
        ml = LD128(rws, vm, 0, SC1);
        CBAR();
      }
      const float M = max8(asf(ml[0])), e = ex(asf(ml[0]) - M), wn = e * rcp(sum8(e * asf(ml[1])));
#pragma unroll
      for (int k = 0; k < 8; ++k) f[k] = asf(__builtin_amdgcn_readlane(asu(wn), k));
      STAMP(3);
      f2 o;
#pragma nounroll
      for (int s = 0; s < SPIN; ++s) {
        v2u g[8];
#pragma unroll
        for (int k = 0; k < 8; ++k) g[k] = LD64(rws, va, k * 768, SC1);
        f2 o0 = __builtin_bit_cast(f2, g[0]) * f[0], o1 = __builtin_bit_cast(f2, g[1]) * f[1];
#pragma unroll
        for (int k = 2; k < 8; k += 2) { o0 += __builtin_bit_cast(f2, g[k]) * f[k]; o1 += __builtin_bit_cast(f2, g[k + 1]) * f[k + 1]; }
        o = o0 + o1;
        CBAR();
        if (!__any(isnan2(o[0], o[1]))) break;
        if ((s & 15) == 15) {
          bool un = false;
#pragma unroll
          for (int k = 0; k < 8; ++k) un |= g[k][0] == SENT || g[k][1] == SENT;
          if (!__any(un)) break;
        }
      }
      if (FINE == 9) STAMP(9);
      const u32 op = pk(o[0], o[1]);
      u32 a[6];
#pragma unroll
      for (int u = 0; u < 6; ++u) a[u] = (u32)__builtin_amdgcn_ds_bpermute(bp + 4 * u, (int)op);
      SCHED();
#pragma unroll
      for (int u = 0; u < 6; ++u) dot_pair(wt[2 * u], wt[2 * u + 1], a[u], acc);
      *(LAS f2*)(LB + la) = red8(acc);
      BARRIER();
      if (w == 0) {
        const f4 y = (*(LAS f4*)(LB + lf) + *(LAS f4*)(LB + lf + 256)) + (*(LAS f4*)(LB + lf + 512) + *(LAS f4*)(LB + lf + 768));
        ST128(__builtin_bit_cast(v4u, y), rws, vb, 0, SC1);
      }
      STAMP(10);
    }
  } else if (jl >= 24) {
    const int gi = 6 * xc + (jl - 24), mat = gi / 24, cgf = gi % 24, G = 40 * w + lane;
    LAS unsigned char* LB = (LAS unsigned char*)L;
    v4u wt[20];
    f4 x, ga, sb, c1;
    float acc[8];
    int bp = 0, vc = 0, ly = 0, ls = 0, la = 0, lf = 0;
    u32 live = 0;
    if (w < 4) {
      const rsrc_t rw = mk(mat ? pwu : pwg, 640 * 1536 * 2), rx = mk(px, 1280);
      const u32 T = 2u * (u32)__builtin_amdgcn_readfirstlane(ev) + 1u;
      const int vo = (160 * w + 20 * r8) * 3072 + cgf * 128 + c8 * 16, vm = lane < 40 ? OFF_M + 5120 + G * 16 : OOB;
      const v2u xp = LD64(rx, G * 8, 0, 0);
      v4u gm[3];
#pragma nounroll
      for (int s = 0; s < SPIN; ++s) {
#pragma unroll
        for (int k = 0; k < 3; ++k) gm[k] = LD128(rws, vm, k * 2560, SC1);
        CBAR();
        if (__all((gbad(gm[0], T) | gbad(gm[1], T) | gbad(gm[2], T)) == 0 || lane >= 40)) break;
      }
#pragma unroll
      for (int t = 0; t < GU_EARLY; ++t) wt[t] = LD128(rw, vo, t * 3072, NT);
      SCHED();
      x = f4{lo(xp[0]), hi(xp[0]), lo(xp[1]), hi(xp[1])};
      ga = f4{lo(gm[0][0]), hi(gm[0][0]), lo(gm[0][1]), hi(gm[0][1])};
      sb = f4{lo(gm[1][0]), hi(gm[1][0]), lo(gm[1][1]), hi(gm[1][1])};
      c1 = f4{rbf(1.f + lo(gm[2][0])), rbf(1.f + hi(gm[2][0])), rbf(1.f + lo(gm[2][1])), rbf(1.f + hi(gm[2][1]))};
      live = lane < 40 ? 0xFFFFFFFFu : 0u;
      bp = 20 * r8; vc = OFF_C + mat * 6144 + cgf * 256 + lane * 4; ly = (L_B + 4 * G) * 4;
      ls = (L_ST + 2 * w + bit5) * 4; la = (L_RED + w * 64 + 8 * c8 + 4 * bit5 + 2 * bit4) * 4; lf = (L_RED + lane) * 4;
      PIN(x); PIN(ga); PIN(sb); PIN(c1); PIN(live); PIN(bp); PIN(vc); PIN(ly); PIN(ls); PIN(la); PIN(lf);
      zero8(acc);
      if (w == 1) stats_duty(c, L, rws, ev);
      lds_wait(L, 4 + (lane & 3), S_ISS);
#pragma unroll
      for (int t = GU_EARLY; t < GU_EARLY + GU_ISS; ++t) wt[t] = LD128(rw, vo, t * 3072, NT);
      SCHED();
      lds_wait(L, 7, S_PUB);
#pragma unroll
      for (int t = GU_EARLY + GU_ISS; t < 20; ++t) wt[t] = LD128(rw, vo, t * 3072, NT);
      SCHED();
      pair_all(wt);
      lds_set(L, w, S_W);
      STAMP(8);
    } else lds_wait(L, w - 4, S_W);
    {
      const int vg = OFF_B + w * 5120 + lane * 16, vg2 = bit5 ? OOB : vg + 2048;
      const int lb = (L_B + 640 * w + 4 * lane) * 4, lb2 = bit5 ? (L_B + 8 * 640 - 128 + 4 * lane) * 4 : lb + 2048;
      f4 y[3];
#pragma nounroll
      for (int s = 0; s < SPIN; ++s) {
        v4u g[6];
#pragma unroll
        for (int k = 0; k < 3; ++k) { g[2 * k] = LD128(rws, k < 2 ? vg : vg2, k < 2 ? k * 1024 : 0, SC1); g[2 * k + 1] = LD128(rws, k < 2 ? vg : vg2, (k < 2 ? k * 1024 : 0) + 2560, SC1); }
        SCHED();
#pragma unroll
        for (int k = 0; k < 3; ++k) y[k] = __builtin_bit_cast(f4, g[2 * k]) + __builtin_bit_cast(f4, g[2 * k + 1]);
        *(LAS f4*)(LB + lb) = y[0]; *(LAS f4*)(LB + lb + 1024) = y[1]; *(LAS f4*)(LB + lb2) = y[2];
        const f4 t = (y[0] + y[1]) + y[2];
        CBAR();
        if (!__any(isnan2(t[0] + t[2], t[1] + t[3]))) break;
        if ((s & 15) == 15) {
          bool un = false;
#pragma unroll
          for (int k = 0; k < 6; ++k) un |= g[k][0] == SENT || g[k][1] == SENT || g[k][2] == SENT || g[k][3] == SENT;
          if (!__any(un)) break;
        }
      }
    }
    if (FINE == 9) STAMP(9);
    BARRIER();
    if (w < 4) {
      if (FINE == 1) STAMP(1);
      f4 y[8];
#pragma unroll
      for (int k = 0; k < 8; ++k) y[k] = *(LAS f4*)(LB + ly + k * 2560);
      x = rbf4(x + rbf4(ga * rbf4(((y[0] + y[1]) + (y[2] + y[3])) + ((y[4] + y[5]) + (y[6] + y[7])))));
      const f4 xx = x * x;
      float st = add32(asf(asu((x[0] + x[2]) + (x[1] + x[3])) & live), asf(asu((xx[0] + xx[2]) + (xx[1] + xx[3])) & live));
      st = add16(st, st); st += dpp<ROR8>(st);
      *(LAS float*)(LB + ls) = sum8(st);
      BARRIER();
      if (FINE == 2) STAMP(2);
      const f4 s4 = *(LAS f4*)(L + L_ST) + *(LAS f4*)(L + L_ST + 4);
      const float mean = (s4[0] + s4[2]) * (1.f / 640.f), var = fmaxf((s4[1] + s4[3]) * (1.f / 640.f) - mean * mean, 0.f);
      const f4 h = rbf4(rbf4(rbf4(x - rbf(mean)) * ln_inv(var)) * c1) + sb;
      const u32 h0 = pk(h[0], h[1]), h1 = pk(h[2], h[3]);
      u32 a[10];
#pragma unroll
      for (int u = 0; u < 5; ++u) { a[2 * u] = (u32)__builtin_amdgcn_ds_bpermute(bp + 4 * u, (int)h0); a[2 * u + 1] = (u32)__builtin_amdgcn_ds_bpermute(bp + 4 * u, (int)h1); }
      SCHED();
#pragma unroll
      for (int u = 0; u < 10; ++u) dot_pair(wt[2 * u], wt[2 * u + 1], a[u], acc);
      *(LAS f2*)(LB + la) = red8(acc);
      BARRIER();
      if (FINE == 3) STAMP(3);
      if (w == 1) {
        float g = rbf((*(LAS float*)(LB + lf) + *(LAS float*)(LB + lf + 512)) + (*(LAS float*)(LB + lf + 256) + *(LAS float*)(LB + lf + 768)));
        if (mat == 0) g = rbf(g * rcp(1.f + __builtin_amdgcn_exp2f(g * (-2.302208198144325f + -0.1029432397800235f * (g * g)))));
        if (WIDE_C) ST128((v4u{asu(g), dppu<0x55>(asu(g)), dppu<0xAA>(asu(g)), dppu<0xFF>(asu(g))}), rws, (lane & 3) ? OOB : vc, 0, SC1);
        else ST32(asu(g), rws, vc, 0, SC1);
      }
      STAMP(10);
    }
  } else {
    if (w < 4) {
      const rsrc_t rw = mk(pwd, 1536 * 640 * 2);
      v4u wt[24];
      const int vo = (384 * w + 16 * (lane >> 2)) * 1280 + di * 64 + (lane & 3) * 16, vo2 = vo + (256 - 8 * (lane >> 2)) * 1280;
#define DLD(t) wt[t] = (t) < 16 ? LD128(rw, vo, (t) * 1280, NT) : LD128(rw, vo2, ((t) - 16) * 1280, NT)
#pragma unroll
      for (int t = 0; t < DN_EARLY; ++t) DLD(t);
      SCHED();
      if (w == 1) stats_duty(c, L, rws, ev);
      lds_wait(L, 4 + (lane & 3), S_ISS);
#pragma unroll
      for (int t = DN_EARLY; t < DN_EARLY + DN_ISS; ++t) DLD(t);
      SCHED();
      lds_wait(L, 7, S_PUB);
#pragma unroll
      for (int t = DN_EARLY + DN_ISS; t < 24; ++t) DLD(t);
      SCHED();
#undef DLD
      pair_all(wt);
      float acc[8];
      zero8(acc);
      int vg = OFF_C + (384 * w + 4 * lane) * 4, vg2 = OFF_C + (384 * w + 256 + 2 * lane) * 4, la = L_RED + w * 32 + 8 * (lane & 3) + 4 * bit5 + 2 * bit4;
      PIN(vg); PIN(vg2); PIN(la);
      STAMP(8);
      if (C_DLY >= 0) { lds_wait(L, 6, 7u); if (C_DLY > 0) __builtin_amdgcn_s_sleep(C_DLY > 0 ? C_DLY : 1); }
      f4 g; f2 g2;
#pragma nounroll
      for (int s = 0; s < SPIN; ++s) {
        const v4u ge = LD128(rws, vg, 0, SC1), up = LD128(rws, vg, 6144, SC1);
        const v2u ge2 = LD64(rws, vg2, 0, SC1), up2 = LD64(rws, vg2, 6144, SC1);
        g = __builtin_bit_cast(f4, ge) * __builtin_bit_cast(f4, up);
        g2 = __builtin_bit_cast(f2, ge2) * __builtin_bit_cast(f2, up2);
        CBAR();
        COUNT(3);
        if (!__any(isnan2((g[0] + g[2]) + g2[0], (g[1] + g[3]) + g2[1]))) break;
        if ((s & 15) == 15) {
          bool un = ge2[0] == SENT || ge2[1] == SENT || up2[0] == SENT || up2[1] == SENT;
#pragma unroll
          for (int k = 0; k < 4; ++k) un |= ge[k] == SENT || up[k] == SENT;
          if (!__any(un)) break;
        }
      }
      if (FINE == 9) STAMP(9);
      const u32 g0 = pk(g[0], g[1]), g1 = pk(g[2], g[3]), gx = pk(g2[0], g2[1]);
      dot_pair(wt[0], wt[1], dppu<0x00>(g0), acc);   dot_pair(wt[2], wt[3], dppu<0x00>(g1), acc);
      dot_pair(wt[4], wt[5], dppu<0x55>(g0), acc);   dot_pair(wt[6], wt[7], dppu<0x55>(g1), acc);
      dot_pair(wt[8], wt[9], dppu<0xAA>(g0), acc);   dot_pair(wt[10], wt[11], dppu<0xAA>(g1), acc);
      dot_pair(wt[12], wt[13], dppu<0xFF>(g0), acc); dot_pair(wt[14], wt[15], dppu<0xFF>(g1), acc);
      dot_pair(wt[16], wt[17], dppu<0x00>(gx), acc); dot_pair(wt[18], wt[19], dppu<0x55>(gx), acc);
      dot_pair(wt[20], wt[21], dppu<0xAA>(gx), acc); dot_pair(wt[22], wt[23], dppu<0xFF>(gx), acc);
      f2 y = red8(acc);
      y[0] += dpp<ROR4>(y[0]); y[1] += dpp<ROR4>(y[1]);
      *(LAS f2*)(L + la) = y;
      BARRIER();
      STAMP(10);
    } else if (w == 6) {
      const rsrc_t rx = mk(px, 1280), ro = mk(pout, 1280);
      const u32 T = 2u * (u32)__builtin_amdgcn_readfirstlane(ev) + 1u;
      const int n = 32 * di + (lane & 31), sh = (n & 1) ? 0 : 16;
      const u32 xp = LD32(rx, (n >> 1) * 4, 0, 0);
      v4u ga, gb;
      u32 g[16];
      lds_wait(L, 7, S_PUB);
#pragma nounroll
      for (int s = 0; s < SPIN; ++s) {
        ga = LD128(rws, OFF_M + 5120 + (n >> 2) * 16, 0, SC1); gb = LD128(rws, OFF_M + 5120 + (n >> 2) * 16, 7680, SC1);
        CBAR();
        if (__all((gbad(ga, T) | gbad(gb, T)) == 0)) break;
      }
#pragma nounroll
      for (int s = 0; s < SPIN; ++s) {
#pragma unroll
        for (int k = 0; k < 16; ++k) g[k] = LD32(rws, bit5 ? OOB : OFF_B + n * 4, k * 2560, SC1);
        bool un = false;
#pragma unroll
        for (int k = 0; k < 16; ++k) un |= g[k] == SENT;
        CBAR();
        if (!__any(un)) break;
        if (C_DLY < 0) __builtin_amdgcn_s_sleep(4);
      }
      if (C_DLY >= 0) lds_set(L, w, 7u);
      float yy[8];
#pragma unroll
      for (int k = 0; k < 8; ++k) yy[k] = asf(g[2 * k]) + asf(g[2 * k + 1]);
      const float y0 = (yy[0] + yy[1]) + (yy[2] + yy[3]), y1 = (yy[4] + yy[5]) + (yy[6] + yy[7]);
      float x1 = rbf(hi(xp << sh) + rbf(hi(((n & 2) ? ga[1] : ga[0]) << sh) * rbf(y0 + y1))), gbf = hi(((n & 2) ? gb[1] : gb[0]) << sh);
      int vs = bit5 ? OOB : n * 2, la = L_RED + (lane & 31);
      PIN(x1); PIN(gbf); PIN(vs); PIN(la);
      STAMP(9);
      BARRIER();
      const float y = (asf(L[la]) + asf(L[la + 64])) + (asf(L[la + 32]) + asf(L[la + 96]));
      __builtin_amdgcn_raw_buffer_store_b16((unsigned short)pk(x1 + rbf(gbf * rbf(y)), 0.f), ro, vs, 0, 0);
      if (di == 0) ST32(ev + 1u, rws, lane == 0 ? OFF_E : OOB, 0, SC1);
      STAMP(10);
    }
  }
  FLUSH();
}

extern "C" void kernel_launch(void* const* d_in, const int* in_sizes, int n_in, void* d_out, int out_size, void* d_ws, size_t ws_size,
                              hipStream_t stream) {
  fused_block<<<256, 512, 0, stream>>>(d_in[0], d_in[1], d_in[2], d_in[3], d_in[4], d_in[6], d_in[7], d_in[8], d_in[9], d_in[10], d_out,
                                       (unsigned char*)d_ws + WSOFF);
}
```
